```python
import torch

def _softmax(x, axis=-1):
    return torch.softmax(x, dim=axis)
def _gelu(x):
    return torch.nn.functional.gelu(x, approximate="tanh")
def _silu(x):
    return torch.nn.functional.silu(x)
def _sigmoid(x):
    return torch.sigmoid(x)
def _layernorm(x, axis=-1, eps=1e-5):
    m = x.mean(dim=axis, keepdim=True)
    v = x.var(dim=axis, keepdim=True, unbiased=False)
    return (x - m) / torch.sqrt(v + eps)
def _scatter_add(tgt, idx, src, axis=0):
    return tgt.clone().index_add_(axis, idx, src)
def _segment_sum(src, seg, n):
    out = torch.zeros((n,) + src.shape[1:], dtype=src.dtype)
    return out.index_add_(0, seg, src)
def _pad(x, pw):
    flat = tuple(v for pair in reversed(pw) for v in pair)
    return torch.nn.functional.pad(x, flat)

kernel_name = 'parallel_sum__gqa_block_cached__dense_activation_relu__decode'

def setup_inputs():

    _w = (lambda a: (lambda t: t.to(torch.bfloat16) if t.is_floating_point() else t)(torch.as_tensor(a)))
    def _A_setup():
        B, Sq, D = 1, 1, 512
        Hq, Hkv, Sk, Dh = 32, 8, 65536, 128
        assert Hq % Hkv == 0, (Hq, Hkv)
        return {
            "x": _w((lambda *s: torch.randn(*s, dtype=torch.bfloat16) * 0.05)(B, Sq, D)),
            "k_cache": _w((lambda *s: torch.randn(*s, dtype=torch.bfloat16) * 3.2)(B, Hkv, Sk, Dh)),
            "v_cache": _w((lambda *s: torch.randn(*s, dtype=torch.bfloat16) * 1.6)(B, Hkv, Sk, Dh)),
            "wq": _w((lambda *s: torch.randn(*s, dtype=torch.bfloat16) * 3.2)(D, Hq * Dh) * (D ** -0.5)),
            "wo": _w((lambda *s: torch.randn(*s, dtype=torch.bfloat16) * 1.6)(Hq * Dh, D) * ((Hq * Dh) ** -0.5)),
        }
    _A_in = _A_setup()

    def _B_setup():
        B, S, D, Dff = 1, 1, 512, 1792
        return {
            "x": _w((lambda *s: torch.randn(*s, dtype=torch.bfloat16) * 0.05)(B, S, D)),
            "w1": _w((lambda *s: torch.randn(*s, dtype=torch.bfloat16) * 0.05)(D, Dff)),
            "w2": _w((lambda *s: torch.randn(*s, dtype=torch.bfloat16) * 0.05)(Dff, D)),
        }
    _B_in = _B_setup()

    return {
        "A_x": _A_in["x"],
        "A_k_cache": _A_in["k_cache"],
        "A_v_cache": _A_in["v_cache"],
        "A_wq": _A_in["wq"],
        "A_wo": _A_in["wo"],
        "B_x": _B_in["x"],
        "B_w1": _B_in["w1"],
        "B_w2": _B_in["w2"],
    }

def reference(A_x, A_k_cache, A_v_cache, A_wq, A_wo, B_x, B_w1, B_w2):
    def _A_ref(x, k_cache, v_cache, wq, wo):
        B, Sq, D = x.shape
        Hq, Dh = 32, 128
        Hkv = k_cache.shape[1]
        rep = Hq // Hkv
        q = (lambda x, *d: x.permute(*d))((x @ wq).reshape(B, Sq, Hq, Dh), 0, 2, 1, 3)
        k = (lambda x, n, axis: torch.repeat_interleave(x, n, dim=axis))(k_cache, rep, axis=1)
        v = (lambda x, n, axis: torch.repeat_interleave(x, n, dim=axis))(v_cache, rep, axis=1)
        scores = torch.einsum("bhqd,bhkd->bhqk", q, k) * (Dh ** -0.5)
        p = _softmax(scores, axis=-1)
        o = torch.einsum("bhqk,bhkd->bhqd", p, v)
        return (lambda x, *d: x.permute(*d))(o, 0, 2, 1, 3).reshape(B, Sq, Hq * Dh) @ wo

    def _B_ref(x, w1, w2):
        h = torch.einsum("bsd,df->bsf", x, w1)
        zero = torch.zeros_like(h)
        h = torch.maximum(h, zero)
        return torch.einsum("bsf,fd->bsd", h, w2)

    x = A_x
    xn = _layernorm(x, axis=-1)
    return x + _A_ref(xn, A_k_cache, A_v_cache, A_wq, A_wo) + _B_ref(xn, B_w1, B_w2)
```

```cpp
#include <hip/hip_runtime.h>
#include <hip/hip_bf16.h>
#include <stdint.h>

#define MIMIC 1
#define USE_NT 1

typedef unsigned short u16;
typedef unsigned u32x4 __attribute__((ext_vector_type(4)));
typedef float f32x4 __attribute__((ext_vector_type(4)));
typedef __bf16 bf16x8 __attribute__((ext_vector_type(8)));

constexpr int DM = 512, HQ = 32, HKV = 8, SK = 65536, DH = 128, DFF = 1792;
constexpr int NSPLIT = 32, NW = 8;
constexpr int KEYS_WG = SK / NSPLIT;
constexpr int KEYS_WAVE = KEYS_WG / NW;
constexpr int NT = KEYS_WAVE / 32;

__device__ __forceinline__ float bf2f(u16 v) { return __uint_as_float(((unsigned)v) << 16); }
__device__ __forceinline__ float bflo(unsigned v) { return __uint_as_float(v << 16); }
__device__ __forceinline__ float bfhi(unsigned v) { return __uint_as_float(v & 0xffff0000u); }
__device__ __forceinline__ u16 f2bf(float f) { __bf16 b = (__bf16)f; return __builtin_bit_cast(u16, b); }
__device__ __forceinline__ float rbf(float f) { return bf2f(f2bf(f)); }
__device__ __forceinline__ unsigned cvtpk(float lo, float hi) {
  typedef __bf16 bf2 __attribute__((ext_vector_type(2)));
  bf2 r; r[0] = (__bf16)lo; r[1] = (__bf16)hi; return __builtin_bit_cast(unsigned, r);
}
template <int CTRL> __device__ __forceinline__ float dpp(float x) {
  return __builtin_bit_cast(float, __builtin_amdgcn_mov_dpp(__builtin_bit_cast(int, x), CTRL, 0xf, 0xf, true));
}
__device__ __forceinline__ float xrow_max(float x) {
  auto s = __builtin_amdgcn_permlane16_swap(__float_as_uint(x), __float_as_uint(x), false, false);
  x = fmaxf(__uint_as_float(s[0]), __uint_as_float(s[1]));
  auto t = __builtin_amdgcn_permlane32_swap(__float_as_uint(x), __float_as_uint(x), false, false);
  return fmaxf(__uint_as_float(t[0]), __uint_as_float(t[1]));
}
__device__ __forceinline__ u32x4 ldkv(const void* p) {
  return __builtin_nontemporal_load(reinterpret_cast<const u32x4*>(p));
}

__device__ __forceinline__ void layernorm_block(const u16* x, float* xn, float* red) {
  const int tid = threadIdx.x;
  float a = bf2f(x[tid]), b = bf2f(x[tid + 256]);
  float s = a + b;
  for (int o = 32; o; o >>= 1) s += __shfl_xor(s, o);
  if ((tid & 63) == 0) red[tid >> 6] = s;
  __syncthreads();
  float mean = (red[0] + red[1] + red[2] + red[3]) * (1.f / 512.f);
  __syncthreads();
  float d0 = a - mean, d1 = b - mean;
  float v = d0 * d0 + d1 * d1;
  for (int o = 32; o; o >>= 1) v += __shfl_xor(v, o);
  if ((tid & 63) == 0) red[tid >> 6] = v;
  __syncthreads();
  float var = (red[0] + red[1] + red[2] + red[3]) * (1.f / 512.f);
  float mb = rbf(mean), vb = rbf(var);
  float den = rbf(sqrtf(rbf(vb + 1e-5f)));
  xn[tid] = rbf(rbf(a - mb) / den);
  xn[tid + 256] = rbf(rbf(b - mb) / den);
  __syncthreads();
}

__global__ void __launch_bounds__(256) k_prep(const u16* x, const u16* wq, const u16* w1, u16* q, float* h) {
  __shared__ float xn[512], red[4], part[32][64];
  layernorm_block(x, xn, red);
  const int tid = threadIdx.x, b = blockIdx.x;
  const bool isq = b < 64;
  const u16* w = isq ? wq + 64 * b : w1 + 64 * (b - 64);
  const int ld = isq ? 4096 : DFF;
  const int c8 = tid & 7, r0 = tid >> 3;
  float acc[8] = {0, 0, 0, 0, 0, 0, 0, 0};
#pragma unroll
  for (int i = 0; i < 16; ++i) {
    const int r = r0 + 32 * i;
    u32x4 v = *reinterpret_cast<const u32x4*>(w + (size_t)r * ld + 8 * c8);
    const float xv = xn[r];
#pragma unroll
    for (int k = 0; k < 4; ++k) { acc[2 * k] += xv * bflo(v[k]); acc[2 * k + 1] += xv * bfhi(v[k]); }
  }
#pragma unroll
  for (int k = 0; k < 8; ++k) part[r0][8 * c8 + k] = acc[k];
  __syncthreads();
  if (tid < 64) {
    float s = 0;
    for (int i = 0; i < 32; ++i) s += part[i][tid];
    if (isq) q[64 * b + tid] = f2bf(s);
    else h[64 * (b - 64) + tid] = fmaxf(rbf(s), 0.f);
  }
}

__global__ void __launch_bounds__(NW * 64) k_attn(const u16* __restrict__ K, const u16* __restrict__ V, const u16* __restrict__ q,
                                                  float* __restrict__ pacc, float* __restrict__ pml) {
  __shared__ float sm_acc[NW][4][128];
  __shared__ float sm_ml[NW][4][2];
  const int b = blockIdx.x, g = b & 7, j = b >> 3;
  const int tid = threadIdx.x, lane = tid & 63, wave = __builtin_amdgcn_readfirstlane(tid >> 6);
  const int lg = lane >> 4, li = lane & 15;
  const u32x4 qb = *reinterpret_cast<const u32x4*>(q + (g * 4 + (li & 3)) * DH + 8 * (4 * lg + (li >> 2)));
  const size_t base = ((size_t)g * SK + (size_t)j * KEYS_WG + (size_t)wave * KEYS_WAVE) * DH * 2;
  const char* kp = (const char*)K + base + (li >> 2) * 256 + lg * 64 + (li & 3) * 16;
  const char* vp = (const char*)V + base + lane * 16;
  constexpr float SC = 0.088388347648318440f * 1.4426950408889634f;
  f32x4 acc[8], accl;
#pragma unroll
  for (int e = 0; e < 8; ++e) acc[e] = f32x4{0, 0, 0, 0};
  accl = f32x4{0, 0, 0, 0};
  float m = -INFINITY;
  u32x4 kb[2][8], vb[2][8];
#pragma unroll
  for (int u = 0; u < 2; ++u) {
#pragma unroll
    for (int t = 0; t < 8; ++t) kb[u][t] = ldkv(kp + (u * 8 + t) * 1024);
#pragma unroll
    for (int t = 0; t < 8; ++t) vb[u][t] = ldkv(vp + (u * 8 + t) * 1024);
  }
  const u32x4 ones = {0x3f803f80u, 0x3f803f80u, 0x3f803f80u, 0x3f803f80u};
#pragma unroll
  for (int it = 0; it < NT; ++it) {
    const int u = it & 1;
    float s[8];
#pragma unroll
    for (int t = 0; t < 8; ++t) {
      f32x4 d = __builtin_amdgcn_mfma_f32_16x16x32_bf16(__builtin_bit_cast(bf16x8, kb[u][t]), __builtin_bit_cast(bf16x8, qb), f32x4{0, 0, 0, 0}, 0, 0, 0);
      float v = d[0] + dpp<0x104>(d[1]);
      v += dpp<0x108>(d[2]);
      v += dpp<0x10c>(d[3]);
      v = rbf(rbf(v) * 0.088388347648318440f) * 1.4426950408889634f;
      s[t] = v;
    }
    if (it + 2 < NT) {
#pragma unroll
      for (int t = 0; t < 8; ++t) kb[u][t] = ldkv(kp + ((it + 2) * 8 + t) * 1024);
    }
    float tm = fmaxf(fmaxf(fmaxf(s[0], s[1]), fmaxf(s[2], s[3])), fmaxf(fmaxf(s[4], s[5]), fmaxf(s[6], s[7])));
    tm = xrow_max(tm);
    const float mn = fmaxf(m, tm);
    const float alpha = __builtin_amdgcn_exp2f(m - mn);
    m = mn;
    u32x4 pb;
#pragma unroll
    for (int k = 0; k < 4; ++k) pb[k] = cvtpk(__builtin_amdgcn_exp2f(s[2 * k] - mn), __builtin_amdgcn_exp2f(s[2 * k + 1] - mn));
#pragma unroll
    for (int e = 0; e < 8; ++e) acc[e] *= alpha;
    accl *= alpha;
#pragma unroll
    for (int e = 0; e < 8; ++e) {
      u32x4 f;
#pragma unroll
      for (int k = 0; k < 4; ++k) {
        const unsigned lo = vb[u][2 * k][e >> 1], hi = vb[u][2 * k + 1][e >> 1];
        f[k] = (e & 1) ? __builtin_amdgcn_perm(hi, lo, 0x07060302u) : __builtin_amdgcn_perm(hi, lo, 0x05040100u);
      }
      acc[e] = __builtin_amdgcn_mfma_f32_16x16x32_bf16(__builtin_bit_cast(bf16x8, f), __builtin_bit_cast(bf16x8, pb), acc[e], 0, 0, 0);
    }
    accl = __builtin_amdgcn_mfma_f32_16x16x32_bf16(__builtin_bit_cast(bf16x8, ones), __builtin_bit_cast(bf16x8, pb), accl, 0, 0, 0);
    if (it + 2 < NT) {
#pragma unroll
      for (int t = 0; t < 8; ++t) vb[u][t] = ldkv(vp + ((it + 2) * 8 + t) * 1024);
    }
  }
  if (li < 4) {
#pragma unroll
    for (int r = 0; r < 4; ++r)
#pragma unroll
      for (int e = 0; e < 8; ++e) sm_acc[wave][li][8 * (4 * lg + r) + e] = acc[e][r];
    if (lg == 0) { sm_ml[wave][li][0] = m; sm_ml[wave][li][1] = accl[0]; }
  }
  __syncthreads();
  {
    const int h = tid >> 7, d = tid & 127;
    float M = -INFINITY;
#pragma unroll
    for (int w = 0; w < NW; ++w) M = fmaxf(M, sm_ml[w][h][0]);
    float L = 0, o = 0;
#pragma unroll
    for (int w = 0; w < NW; ++w) { const float wt = __builtin_amdgcn_exp2f(sm_ml[w][h][0] - M); L += wt * sm_ml[w][h][1]; o += wt * sm_acc[w][h][d]; }
    const int slot = (g * 4 + h) * NSPLIT + j;
    pacc[slot * 128 + d] = o;
    if (d == 0) { pml[slot * 2] = M; pml[slot * 2 + 1] = L; }
  }
}

__global__ void __launch_bounds__(256) k_comb(const float* pacc, const float* pml, const float* h, const u16* wo, const u16* w2, float* ypart) {
  __shared__ float o[128];
  __shared__ float part[4][512];
  const int b = blockIdx.x, tid = threadIdx.x;
  const u16* w;
  if (b < 32) {
    if (tid < 128) {
      float M = -INFINITY;
      for (int s = 0; s < NSPLIT; ++s) M = fmaxf(M, pml[(b * NSPLIT + s) * 2]);
      float L = 0, a = 0;
      for (int s = 0; s < NSPLIT; ++s) { const float wt = __builtin_amdgcn_exp2f(pml[(b * NSPLIT + s) * 2] - M); L += wt * pml[(b * NSPLIT + s) * 2 + 1]; a += wt * pacc[(b * NSPLIT + s) * 128 + tid]; }
      o[tid] = rbf(a / L);
    }
    w = wo + (size_t)b * 128 * 512;
  } else {
    if (tid < 128) o[tid] = h[(b - 32) * 128 + tid];
    w = w2 + (size_t)(b - 32) * 128 * 512;
  }
  __syncthreads();
  const int c = tid & 63, rg = tid >> 6;
  float acc[8] = {0, 0, 0, 0, 0, 0, 0, 0};
#pragma unroll 8
  for (int i = 0; i < 32; ++i) {
    const int r = rg * 32 + i;
    u32x4 v = *reinterpret_cast<const u32x4*>(w + (size_t)r * 512 + 8 * c);
    const float xv = o[r];
#pragma unroll
    for (int k = 0; k < 4; ++k) { acc[2 * k] += xv * bflo(v[k]); acc[2 * k + 1] += xv * bfhi(v[k]); }
  }
#pragma unroll
  for (int k = 0; k < 8; ++k) part[rg][8 * c + k] = acc[k];
  __syncthreads();
  for (int i = tid; i < 512; i += 256) ypart[b * 512 + i] = part[0][i] + part[1][i] + part[2][i] + part[3][i];
}

__global__ void __launch_bounds__(512) k_final(const float* ypart, const u16* x, u16* out) {
  const int t = threadIdx.x;
  float a = 0, f = 0;
  for (int b = 0; b < 32; ++b) a += ypart[b * 512 + t];
  for (int b = 32; b < 46; ++b) f += ypart[b * 512 + t];
  out[t] = f2bf(rbf(bf2f(x[t]) + rbf(a)) + rbf(f));
}

extern "C" void kernel_launch(void* const* d_in, const int* in_sizes, int n_in, void* d_out, int out_size, void* d_ws, size_t ws_size,
                              hipStream_t stream) {
  const u16* x = (const u16*)d_in[0];
  const u16* K = (const u16*)d_in[1];
  const u16* V = (const u16*)d_in[2];
  const u16* wq = (const u16*)d_in[3];
  const u16* wo = (const u16*)d_in[4];
  const u16* w1 = (const u16*)d_in[6];
  const u16* w2 = (const u16*)d_in[7];
  char* ws = (char*)d_ws;
  u16* q = (u16*)ws;
  float* h = (float*)(ws + 16384);
  float* pml = (float*)(ws + 32768);
  float* pacc = (float*)(ws + 65536);
  float* ypart = (float*)(ws + 65536 + 524288);
  k_prep<<<92, 256, 0, stream>>>(x, wq, w1, q, h);
  k_attn<<<256, NW * 64, 0, stream>>>(K, V, q, pacc, pml);
  k_comb<<<46, 256, 0, stream>>>(pacc, pml, h, wo, w2, ypart);
  k_final<<<1, 512, 0, stream>>>(ypart, x, (u16*)d_out);
}
```

```cpp
#define KNT 1
#define VNT 1
#include <hip/hip_runtime.h>
#include <hip/hip_bf16.h>
#include <stdint.h>

#define MIMIC 1
#define USE_NT 1

typedef unsigned short u16;
typedef unsigned u32x4 __attribute__((ext_vector_type(4)));
typedef float f32x4 __attribute__((ext_vector_type(4)));
typedef __bf16 bf16x8 __attribute__((ext_vector_type(8)));

constexpr int DM = 512, HQ = 32, HKV = 8, SK = 65536, DH = 128, DFF = 1792;
constexpr int NSPLIT = 32, NW = 8;
constexpr int KEYS_WG = SK / NSPLIT;
constexpr int KEYS_WAVE = KEYS_WG / NW;
constexpr int NT = KEYS_WAVE / 32;

__device__ __forceinline__ float bf2f(u16 v) { return __uint_as_float(((unsigned)v) << 16); }
__device__ __forceinline__ float bflo(unsigned v) { return __uint_as_float(v << 16); }
__device__ __forceinline__ float bfhi(unsigned v) { return __uint_as_float(v & 0xffff0000u); }
__device__ __forceinline__ u16 f2bf(float f) { __bf16 b = (__bf16)f; return __builtin_bit_cast(u16, b); }
__device__ __forceinline__ float rbf(float f) { return bf2f(f2bf(f)); }
__device__ __forceinline__ unsigned cvtpk(float lo, float hi) {
  typedef __bf16 bf2 __attribute__((ext_vector_type(2)));
  bf2 r; r[0] = (__bf16)lo; r[1] = (__bf16)hi; return __builtin_bit_cast(unsigned, r);
}
template <int CTRL> __device__ __forceinline__ float dpp(float x) {
  return __builtin_bit_cast(float, __builtin_amdgcn_mov_dpp(__builtin_bit_cast(int, x), CTRL, 0xf, 0xf, true));
}
__device__ __forceinline__ float xrow_max(float x) {
  auto s = __builtin_amdgcn_permlane16_swap(__float_as_uint(x), __float_as_uint(x), false, false);
  x = fmaxf(__uint_as_float(s[0]), __uint_as_float(s[1]));
  auto t = __builtin_amdgcn_permlane32_swap(__float_as_uint(x), __float_as_uint(x), false, false);
  return fmaxf(__uint_as_float(t[0]), __uint_as_float(t[1]));
}
template <int NTP> __device__ __forceinline__ u32x4 ldp(const void* p) {
  if constexpr (NTP) return __builtin_nontemporal_load(reinterpret_cast<const u32x4*>(p));
  else return *reinterpret_cast<const u32x4*>(p);
}

__device__ __forceinline__ void layernorm_block(const u16* x, float* xn, float* red) {
  const int tid = threadIdx.x;
  float a = bf2f(x[tid]), b = bf2f(x[tid + 256]);
  float s = a + b;
  for (int o = 32; o; o >>= 1) s += __shfl_xor(s, o);
  if ((tid & 63) == 0) red[tid >> 6] = s;
  __syncthreads();
  float mean = (red[0] + red[1] + red[2] + red[3]) * (1.f / 512.f);
  __syncthreads();
  float d0 = a - mean, d1 = b - mean;
  float v = d0 * d0 + d1 * d1;
  for (int o = 32; o; o >>= 1) v += __shfl_xor(v, o);
  if ((tid & 63) == 0) red[tid >> 6] = v;
  __syncthreads();
  float var = (red[0] + red[1] + red[2] + red[3]) * (1.f / 512.f);
  float mb = rbf(mean), vb = rbf(var);
  float den = rbf(sqrtf(rbf(vb + 1e-5f)));
  xn[tid] = rbf(rbf(a - mb) / den);
  xn[tid + 256] = rbf(rbf(b - mb) / den);
  __syncthreads();
}

__global__ void __launch_bounds__(256) k_prep(const u16* x, const u16* wq, const u16* w1, u16* q, float* h) {
  __shared__ float xn[512], red[4], part[32][64];
  layernorm_block(x, xn, red);
  const int tid = threadIdx.x, b = blockIdx.x;
  const bool isq = b < 64;
  const u16* w = isq ? wq + 64 * b : w1 + 64 * (b - 64);
  const int ld = isq ? 4096 : DFF;
  const int c8 = tid & 7, r0 = tid >> 3;
  float acc[8] = {0, 0, 0, 0, 0, 0, 0, 0};
#pragma unroll
  for (int i = 0; i < 16; ++i) {
    const int r = r0 + 32 * i;
    u32x4 v = *reinterpret_cast<const u32x4*>(w + (size_t)r * ld + 8 * c8);
    const float xv = xn[r];
#pragma unroll
    for (int k = 0; k < 4; ++k) { acc[2 * k] += xv * bflo(v[k]); acc[2 * k + 1] += xv * bfhi(v[k]); }
  }
#pragma unroll
  for (int k = 0; k < 8; ++k) part[r0][8 * c8 + k] = acc[k];
  __syncthreads();
  if (tid < 64) {
    float s = 0;
    for (int i = 0; i < 32; ++i) s += part[i][tid];
    if (isq) q[64 * b + tid] = f2bf(s);
    else h[64 * (b - 64) + tid] = fmaxf(rbf(s), 0.f);
  }
}

__global__ void __launch_bounds__(NW * 64) k_attn(const u16* __restrict__ K, const u16* __restrict__ V, const u16* __restrict__ q,
                                                  float* __restrict__ pacc, float* __restrict__ pml) {
  __shared__ float sm_acc[NW][4][128];
  __shared__ float sm_ml[NW][4][2];
  const int b = blockIdx.x, g = b & 7, j = b >> 3;
  const int tid = threadIdx.x, lane = tid & 63, wave = __builtin_amdgcn_readfirstlane(tid >> 6);
  const int lg = lane >> 4, li = lane & 15;
  const u32x4 qb = *reinterpret_cast<const u32x4*>(q + (g * 4 + (li & 3)) * DH + 8 * (4 * lg + (li >> 2)));
  const size_t base = ((size_t)g * SK + (size_t)j * KEYS_WG + (size_t)wave * KEYS_WAVE) * DH * 2;
  const char* kp = (const char*)K + base + (li >> 2) * 256 + lg * 64 + (li & 3) * 16;
  const char* vp = (const char*)V + base + lane * 16;
  constexpr float SC = 0.088388347648318440f * 1.4426950408889634f;
  f32x4 acc[8], accl;
#pragma unroll
  for (int e = 0; e < 8; ++e) acc[e] = f32x4{0, 0, 0, 0};
  accl = f32x4{0, 0, 0, 0};
  float m = -INFINITY;
  u32x4 kb[2][8], vb[2][8];
#pragma unroll
  for (int u = 0; u < 2; ++u) {
#pragma unroll
    for (int t = 0; t < 8; ++t) kb[u][t] = ldp<KNT>(kp + (u * 8 + t) * 1024);
#pragma unroll
    for (int t = 0; t < 8; ++t) vb[u][t] = ldp<VNT>(vp + (u * 8 + t) * 1024);
  }
  const u32x4 ones = {0x3f803f80u, 0x3f803f80u, 0x3f803f80u, 0x3f803f80u};
#pragma unroll
  for (int it = 0; it < NT; ++it) {
    const int u = it & 1;
    float s[8];
#pragma unroll
    for (int t = 0; t < 8; ++t) {
      f32x4 d = __builtin_amdgcn_mfma_f32_16x16x32_bf16(__builtin_bit_cast(bf16x8, kb[u][t]), __builtin_bit_cast(bf16x8, qb), f32x4{0, 0, 0, 0}, 0, 0, 0);
      float v = d[0] + dpp<0x104>(d[1]);
      v += dpp<0x108>(d[2]);
      v += dpp<0x10c>(d[3]);
      v = rbf(rbf(v) * 0.088388347648318440f) * 1.4426950408889634f;
      s[t] = v;
    }
    if (it + 2 < NT) {
#pragma unroll
      for (int t = 0; t < 8; ++t) kb[u][t] = ldp<KNT>(kp + ((it + 2) * 8 + t) * 1024);
    }
    float tm = fmaxf(fmaxf(fmaxf(s[0], s[1]), fmaxf(s[2], s[3])), fmaxf(fmaxf(s[4], s[5]), fmaxf(s[6], s[7])));
    tm = xrow_max(tm);
    const float mn = fmaxf(m, tm);
    const float alpha = __builtin_amdgcn_exp2f(m - mn);
    m = mn;
    u32x4 pb;
#pragma unroll
    for (int k = 0; k < 4; ++k) pb[k] = cvtpk(__builtin_amdgcn_exp2f(s[2 * k] - mn), __builtin_amdgcn_exp2f(s[2 * k + 1] - mn));
#pragma unroll
    for (int e = 0; e < 8; ++e) acc[e] *= alpha;
    accl *= alpha;
#pragma unroll
    for (int e = 0; e < 8; ++e) {
      u32x4 f;
#pragma unroll
      for (int k = 0; k < 4; ++k) {
        const unsigned lo = vb[u][2 * k][e >> 1], hi = vb[u][2 * k + 1][e >> 1];
        f[k] = (e & 1) ? __builtin_amdgcn_perm(hi, lo, 0x07060302u) : __builtin_amdgcn_perm(hi, lo, 0x05040100u);
      }
      acc[e] = __builtin_amdgcn_mfma_f32_16x16x32_bf16(__builtin_bit_cast(bf16x8, f), __builtin_bit_cast(bf16x8, pb), acc[e], 0, 0, 0);
    }
    accl = __builtin_amdgcn_mfma_f32_16x16x32_bf16(__builtin_bit_cast(bf16x8, ones), __builtin_bit_cast(bf16x8, pb), accl, 0, 0, 0);
    if (it + 2 < NT) {
#pragma unroll
      for (int t = 0; t < 8; ++t) vb[u][t] = ldp<VNT>(vp + ((it + 2) * 8 + t) * 1024);
    }
  }
  if (li < 4) {
#pragma unroll
    for (int r = 0; r < 4; ++r)
#pragma unroll
      for (int e = 0; e < 8; ++e) sm_acc[wave][li][8 * (4 * lg + r) + e] = acc[e][r];
    if (lg == 0) { sm_ml[wave][li][0] = m; sm_ml[wave][li][1] = accl[0]; }
  }
  __syncthreads();
  {
    const int h = tid >> 7, d = tid & 127;
    float M = -INFINITY;
#pragma unroll
    for (int w = 0; w < NW; ++w) M = fmaxf(M, sm_ml[w][h][0]);
    float L = 0, o = 0;
#pragma unroll
    for (int w = 0; w < NW; ++w) { const float wt = __builtin_amdgcn_exp2f(sm_ml[w][h][0] - M); L += wt * sm_ml[w][h][1]; o += wt * sm_acc[w][h][d]; }
    const int slot = (g * 4 + h) * NSPLIT + j;
    pacc[slot * 128 + d] = o;
    if (d == 0) { pml[slot * 2] = M; pml[slot * 2 + 1] = L; }
  }
}

__global__ void __launch_bounds__(256) k_comb(const float* pacc, const float* pml, const float* h, const u16* wo, const u16* w2, float* ypart) {
  __shared__ float o[128];
  __shared__ float part[4][512];
  const int b = blockIdx.x, tid = threadIdx.x;
  const u16* w;
  if (b < 32) {
    if (tid < 128) {
      float M = -INFINITY;
      for (int s = 0; s < NSPLIT; ++s) M = fmaxf(M, pml[(b * NSPLIT + s) * 2]);
      float L = 0, a = 0;
      for (int s = 0; s < NSPLIT; ++s) { const float wt = __builtin_amdgcn_exp2f(pml[(b * NSPLIT + s) * 2] - M); L += wt * pml[(b * NSPLIT + s) * 2 + 1]; a += wt * pacc[(b * NSPLIT + s) * 128 + tid]; }
      o[tid] = rbf(a / L);
    }
    w = wo + (size_t)b * 128 * 512;
  } else {
    if (tid < 128) o[tid] = h[(b - 32) * 128 + tid];
    w = w2 + (size_t)(b - 32) * 128 * 512;
  }
  __syncthreads();
  const int c = tid & 63, rg = tid >> 6;
  float acc[8] = {0, 0, 0, 0, 0, 0, 0, 0};
#pragma unroll 8
  for (int i = 0; i < 32; ++i) {
    const int r = rg * 32 + i;
    u32x4 v = *reinterpret_cast<const u32x4*>(w + (size_t)r * 512 + 8 * c);
    const float xv = o[r];
#pragma unroll
    for (int k = 0; k < 4; ++k) { acc[2 * k] += xv * bflo(v[k]); acc[2 * k + 1] += xv * bfhi(v[k]); }
  }
#pragma unroll
  for (int k = 0; k < 8; ++k) part[rg][8 * c + k] = acc[k];
  __syncthreads();
  for (int i = tid; i < 512; i += 256) ypart[b * 512 + i] = part[0][i] + part[1][i] + part[2][i] + part[3][i];
}

__global__ void __launch_bounds__(512) k_final(const float* ypart, const u16* x, u16* out) {
  const int t = threadIdx.x;
  float a = 0, f = 0;
  for (int b = 0; b < 32; ++b) a += ypart[b * 512 + t];
  for (int b = 32; b < 46; ++b) f += ypart[b * 512 + t];
  out[t] = f2bf(rbf(bf2f(x[t]) + rbf(a)) + rbf(f));
}

extern "C" void kernel_launch(void* const* d_in, const int* in_sizes, int n_in, void* d_out, int out_size, void* d_ws, size_t ws_size,
                              hipStream_t stream) {
  const u16* x = (const u16*)d_in[0];
  const u16* K = (const u16*)d_in[1];
  const u16* V = (const u16*)d_in[2];
  const u16* wq = (const u16*)d_in[3];
  const u16* wo = (const u16*)d_in[4];
  const u16* w1 = (const u16*)d_in[6];
  const u16* w2 = (const u16*)d_in[7];
  char* ws = (char*)d_ws;
  u16* q = (u16*)ws;
  float* h = (float*)(ws + 16384);
  float* pml = (float*)(ws + 32768);
  float* pacc = (float*)(ws + 65536);
  float* ypart = (float*)(ws + 65536 + 524288);
  k_prep<<<92, 256, 0, stream>>>(x, wq, w1, q, h);
  k_attn<<<256, NW * 64, 0, stream>>>(K, V, q, pacc, pml);
  k_comb<<<46, 256, 0, stream>>>(pacc, pml, h, wo, w2, ypart);
  k_final<<<1, 512, 0, stream>>>(ypart, x, (u16*)d_out);
}
```

```cpp
#define PAT 1
#define MAP 0
#define KNT 1
#define VNT 1
#include <hip/hip_runtime.h>
#include <hip/hip_bf16.h>
#include <stdint.h>

#define MIMIC 1
#define USE_NT 1

typedef unsigned short u16;
typedef unsigned u32x4 __attribute__((ext_vector_type(4)));
typedef float f32x4 __attribute__((ext_vector_type(4)));
typedef __bf16 bf16x8 __attribute__((ext_vector_type(8)));

constexpr int DM = 512, HQ = 32, HKV = 8, SK = 65536, DH = 128, DFF = 1792;
constexpr int NSPLIT = 32, NW = 8;
constexpr int KEYS_WG = SK / NSPLIT;
constexpr int KEYS_WAVE = KEYS_WG / NW;
constexpr int NT = KEYS_WAVE / 32;

__device__ __forceinline__ float bf2f(u16 v) { return __uint_as_float(((unsigned)v) << 16); }
__device__ __forceinline__ float bflo(unsigned v) { return __uint_as_float(v << 16); }
__device__ __forceinline__ float bfhi(unsigned v) { return __uint_as_float(v & 0xffff0000u); }
__device__ __forceinline__ u16 f2bf(float f) { __bf16 b = (__bf16)f; return __builtin_bit_cast(u16, b); }
__device__ __forceinline__ float rbf(float f) { return bf2f(f2bf(f)); }
__device__ __forceinline__ unsigned cvtpk(float lo, float hi) {
  typedef __bf16 bf2 __attribute__((ext_vector_type(2)));
  bf2 r; r[0] = (__bf16)lo; r[1] = (__bf16)hi; return __builtin_bit_cast(unsigned, r);
}
template <int CTRL> __device__ __forceinline__ float dpp(float x) {
  return __builtin_bit_cast(float, __builtin_amdgcn_mov_dpp(__builtin_bit_cast(int, x), CTRL, 0xf, 0xf, true));
}
__device__ __forceinline__ float xrow_max(float x) {
  auto s = __builtin_amdgcn_permlane16_swap(__float_as_uint(x), __float_as_uint(x), false, false);
  x = fmaxf(__uint_as_float(s[0]), __uint_as_float(s[1]));
  auto t = __builtin_amdgcn_permlane32_swap(__float_as_uint(x), __float_as_uint(x), false, false);
  return fmaxf(__uint_as_float(t[0]), __uint_as_float(t[1]));
}
template <int NTP> __device__ __forceinline__ u32x4 ldp(const void* p) {
  if constexpr (NTP) return __builtin_nontemporal_load(reinterpret_cast<const u32x4*>(p));
  else return *reinterpret_cast<const u32x4*>(p);
}

__device__ __forceinline__ void layernorm_block(const u16* x, float* xn, float* red) {
  const int tid = threadIdx.x;
  float a = bf2f(x[tid]), b = bf2f(x[tid + 256]);
  float s = a + b;
  for (int o = 32; o; o >>= 1) s += __shfl_xor(s, o);
  if ((tid & 63) == 0) red[tid >> 6] = s;
  __syncthreads();
  float mean = (red[0] + red[1] + red[2] + red[3]) * (1.f / 512.f);
  __syncthreads();
  float d0 = a - mean, d1 = b - mean;
  float v = d0 * d0 + d1 * d1;
  for (int o = 32; o; o >>= 1) v += __shfl_xor(v, o);
  if ((tid & 63) == 0) red[tid >> 6] = v;
  __syncthreads();
  float var = (red[0] + red[1] + red[2] + red[3]) * (1.f / 512.f);
  float mb = rbf(mean), vb = rbf(var);
  float den = rbf(sqrtf(rbf(vb + 1e-5f)));
  xn[tid] = rbf(rbf(a - mb) / den);
  xn[tid + 256] = rbf(rbf(b - mb) / den);
  __syncthreads();
}

__global__ void __launch_bounds__(256) k_prep(const u16* x, const u16* wq, const u16* w1, u16* q, float* h) {
  __shared__ float xn[512], red[4], part[32][64];
  layernorm_block(x, xn, red);
  const int tid = threadIdx.x, b = blockIdx.x;
  const bool isq = b < 64;
  const u16* w = isq ? wq + 64 * b : w1 + 64 * (b - 64);
  const int ld = isq ? 4096 : DFF;
  const int c8 = tid & 7, r0 = tid >> 3;
  float acc[8] = {0, 0, 0, 0, 0, 0, 0, 0};
#pragma unroll
  for (int i = 0; i < 16; ++i) {
    const int r = r0 + 32 * i;
    u32x4 v = *reinterpret_cast<const u32x4*>(w + (size_t)r * ld + 8 * c8);
    const float xv = xn[r];
#pragma unroll
    for (int k = 0; k < 4; ++k) { acc[2 * k] += xv * bflo(v[k]); acc[2 * k + 1] += xv * bfhi(v[k]); }
  }
#pragma unroll
  for (int k = 0; k < 8; ++k) part[r0][8 * c8 + k] = acc[k];
  __syncthreads();
  if (tid < 64) {
    float s = 0;
    for (int i = 0; i < 32; ++i) s += part[i][tid];
    if (isq) q[64 * b + tid] = f2bf(s);
    else h[64 * (b - 64) + tid] = fmaxf(rbf(s), 0.f);
  }
}

__global__ void __launch_bounds__(NW * 64) k_attn(const u16* __restrict__ K, const u16* __restrict__ V, const u16* __restrict__ q,
                                                  float* __restrict__ pacc, float* __restrict__ pml) {
  __shared__ float sm_acc[NW][4][128];
  __shared__ float sm_ml[NW][4][2];
  const int b = blockIdx.x, g = MAP == 0 ? (b & 7) : (b >> 5), j = MAP == 0 ? (b >> 3) : (b & 31);
  const int tid = threadIdx.x, lane = tid & 63, wave = __builtin_amdgcn_readfirstlane(tid >> 6);
  const int lg = lane >> 4, li = lane & 15;
  const u32x4 qb = *reinterpret_cast<const u32x4*>(q + (g * 4 + (li & 3)) * DH + 8 * (4 * lg + (li >> 2)));
  const size_t base = (size_t)g * SK * 256 + (PAT == 0 ? ((size_t)j * KEYS_WG + (size_t)wave * KEYS_WAVE) * 256 : PAT == 1 ? ((size_t)j * KEYS_WG * 256 + wave * 8192) : (size_t)(j * NW + wave) * 8192);
  constexpr size_t TS = PAT == 0 ? 8192 : PAT == 1 ? 8192 * NW : 8192 * NW * 32;
  const char* kp = (const char*)K + base + (li >> 2) * 256 + lg * 64 + (li & 3) * 16;
  const char* vp = (const char*)V + base + lane * 16;
  constexpr float SC = 0.088388347648318440f * 1.4426950408889634f;
  f32x4 acc[8], accl;
#pragma unroll
  for (int e = 0; e < 8; ++e) acc[e] = f32x4{0, 0, 0, 0};
  accl = f32x4{0, 0, 0, 0};
  float m = -INFINITY;
  u32x4 kb[2][8], vb[2][8];
#pragma unroll
  for (int u = 0; u < 2; ++u) {
#pragma unroll
    for (int t = 0; t < 8; ++t) kb[u][t] = ldp<KNT>(kp + u * TS + t * 1024);
#pragma unroll
    for (int t = 0; t < 8; ++t) vb[u][t] = ldp<VNT>(vp + u * TS + t * 1024);
  }
  const u32x4 ones = {0x3f803f80u, 0x3f803f80u, 0x3f803f80u, 0x3f803f80u};
#pragma unroll
  for (int it = 0; it < NT; ++it) {
    const int u = it & 1;
    float s[8];
#pragma unroll
    for (int t = 0; t < 8; ++t) {
      f32x4 d = __builtin_amdgcn_mfma_f32_16x16x32_bf16(__builtin_bit_cast(bf16x8, kb[u][t]), __builtin_bit_cast(bf16x8, qb), f32x4{0, 0, 0, 0}, 0, 0, 0);
      float v = d[0] + dpp<0x104>(d[1]);
      v += dpp<0x108>(d[2]);
      v += dpp<0x10c>(d[3]);
      v = rbf(rbf(v) * 0.088388347648318440f) * 1.4426950408889634f;
      s[t] = v;
    }
    if (it + 2 < NT) {
#pragma unroll
      for (int t = 0; t < 8; ++t) kb[u][t] = ldp<KNT>(kp + (it + 2) * TS + t * 1024);
    }
    float tm = fmaxf(fmaxf(fmaxf(s[0], s[1]), fmaxf(s[2], s[3])), fmaxf(fmaxf(s[4], s[5]), fmaxf(s[6], s[7])));
    tm = xrow_max(tm);
    const float mn = fmaxf(m, tm);
    const float alpha = __builtin_amdgcn_exp2f(m - mn);
    m = mn;
    u32x4 pb;
#pragma unroll
    for (int k = 0; k < 4; ++k) pb[k] = cvtpk(__builtin_amdgcn_exp2f(s[2 * k] - mn), __builtin_amdgcn_exp2f(s[2 * k + 1] - mn));
#pragma unroll
    for (int e = 0; e < 8; ++e) acc[e] *= alpha;
    accl *= alpha;
#pragma unroll
    for (int e = 0; e < 8; ++e) {
      u32x4 f;
#pragma unroll
      for (int k = 0; k < 4; ++k) {
        const unsigned lo = vb[u][2 * k][e >> 1], hi = vb[u][2 * k + 1][e >> 1];
        f[k] = (e & 1) ? __builtin_amdgcn_perm(hi, lo, 0x07060302u) : __builtin_amdgcn_perm(hi, lo, 0x05040100u);
      }
      acc[e] = __builtin_amdgcn_mfma_f32_16x16x32_bf16(__builtin_bit_cast(bf16x8, f), __builtin_bit_cast(bf16x8, pb), acc[e], 0, 0, 0);
    }
    accl = __builtin_amdgcn_mfma_f32_16x16x32_bf16(__builtin_bit_cast(bf16x8, ones), __builtin_bit_cast(bf16x8, pb), accl, 0, 0, 0);
    if (it + 2 < NT) {
#pragma unroll
      for (int t = 0; t < 8; ++t) vb[u][t] = ldp<VNT>(vp + (it + 2) * TS + t * 1024);
    }
  }
  if (li < 4) {
#pragma unroll
    for (int r = 0; r < 4; ++r)
#pragma unroll
      for (int e = 0; e < 8; ++e) sm_acc[wave][li][8 * (4 * lg + r) + e] = acc[e][r];
    if (lg == 0) { sm_ml[wave][li][0] = m; sm_ml[wave][li][1] = accl[0]; }
  }
  __syncthreads();
  {
    const int h = tid >> 7, d = tid & 127;
    float M = -INFINITY;
#pragma unroll
    for (int w = 0; w < NW; ++w) M = fmaxf(M, sm_ml[w][h][0]);
    float L = 0, o = 0;
#pragma unroll
    for (int w = 0; w < NW; ++w) { const float wt = __builtin_amdgcn_exp2f(sm_ml[w][h][0] - M); L += wt * sm_ml[w][h][1]; o += wt * sm_acc[w][h][d]; }
    const int slot = (g * 4 + h) * NSPLIT + j;
    pacc[slot * 128 + d] = o;
    if (d == 0) { pml[slot * 2] = M; pml[slot * 2 + 1] = L; }
  }
}

__global__ void __launch_bounds__(256) k_comb(const float* pacc, const float* pml, const float* h, const u16* wo, const u16* w2, float* ypart) {
  __shared__ float o[128];
  __shared__ float part[4][512];
  const int b = blockIdx.x, tid = threadIdx.x;
  const u16* w;
  if (b < 32) {
    if (tid < 128) {
      float M = -INFINITY;
      for (int s = 0; s < NSPLIT; ++s) M = fmaxf(M, pml[(b * NSPLIT + s) * 2]);
      float L = 0, a = 0;
      for (int s = 0; s < NSPLIT; ++s) { const float wt = __builtin_amdgcn_exp2f(pml[(b * NSPLIT + s) * 2] - M); L += wt * pml[(b * NSPLIT + s) * 2 + 1]; a += wt * pacc[(b * NSPLIT + s) * 128 + tid]; }
      o[tid] = rbf(a / L);
    }
    w = wo + (size_t)b * 128 * 512;
  } else {
    if (tid < 128) o[tid] = h[(b - 32) * 128 + tid];
    w = w2 + (size_t)(b - 32) * 128 * 512;
  }
  __syncthreads();
  const int c = tid & 63, rg = tid >> 6;
  float acc[8] = {0, 0, 0, 0, 0, 0, 0, 0};
#pragma unroll 8
  for (int i = 0; i < 32; ++i) {
    const int r = rg * 32 + i;
    u32x4 v = *reinterpret_cast<const u32x4*>(w + (size_t)r * 512 + 8 * c);
    const float xv = o[r];
#pragma unroll
    for (int k = 0; k < 4; ++k) { acc[2 * k] += xv * bflo(v[k]); acc[2 * k + 1] += xv * bfhi(v[k]); }
  }
#pragma unroll
  for (int k = 0; k < 8; ++k) part[rg][8 * c + k] = acc[k];
  __syncthreads();
  for (int i = tid; i < 512; i += 256) ypart[b * 512 + i] = part[0][i] + part[1][i] + part[2][i] + part[3][i];
}

__global__ void __launch_bounds__(512) k_final(const float* ypart, const u16* x, u16* out) {
  const int t = threadIdx.x;
  float a = 0, f = 0;
  for (int b = 0; b < 32; ++b) a += ypart[b * 512 + t];
  for (int b = 32; b < 46; ++b) f += ypart[b * 512 + t];
  out[t] = f2bf(rbf(bf2f(x[t]) + rbf(a)) + rbf(f));
}

extern "C" void kernel_launch(void* const* d_in, const int* in_sizes, int n_in, void* d_out, int out_size, void* d_ws, size_t ws_size,
                              hipStream_t stream) {
  const u16* x = (const u16*)d_in[0];
  const u16* K = (const u16*)d_in[1];
  const u16* V = (const u16*)d_in[2];
  const u16* wq = (const u16*)d_in[3];
  const u16* wo = (const u16*)d_in[4];
  const u16* w1 = (const u16*)d_in[6];
  const u16* w2 = (const u16*)d_in[7];
  char* ws = (char*)d_ws;
  u16* q = (u16*)ws;
  float* h = (float*)(ws + 16384);
  float* pml = (float*)(ws + 32768);
  float* pacc = (float*)(ws + 65536);
  float* ypart = (float*)(ws + 65536 + 524288);
  k_prep<<<92, 256, 0, stream>>>(x, wq, w1, q, h);
  k_attn<<<256, NW * 64, 0, stream>>>(K, V, q, pacc, pml);
  k_comb<<<46, 256, 0, stream>>>(pacc, pml, h, wo, w2, ypart);
  k_final<<<1, 512, 0, stream>>>(ypart, x, (u16*)d_out);
}
```

```cpp
#include <hip/hip_runtime.h>
#include <hip/hip_bf16.h>
#include <stdint.h>

#define WIN 6
#define FFNJ 28
#define DRY 1
#define E2SLEEP 1
#define QSLEEP 8
#define E1SLEEP 1
#define E2X4 1
#define STAMPS 0

typedef unsigned short u16;
typedef unsigned long long u64;
typedef unsigned u32x4 __attribute__((ext_vector_type(4)));
typedef unsigned u32x2 __attribute__((ext_vector_type(2)));
typedef float f32x4 __attribute__((ext_vector_type(4)));
typedef float f32x2 __attribute__((ext_vector_type(2)));
typedef __bf16 bf16x8 __attribute__((ext_vector_type(8)));
typedef __bf16 bf16x2 __attribute__((ext_vector_type(2)));
typedef __amdgpu_buffer_rsrc_t rsrc_t;
#define LAS __attribute__((address_space(3)))

constexpr int DM = 512, SK = 65536, DH = 128, DFF = 1792;
constexpr int NT = 16;
constexpr unsigned HEADB = SK * DH * 2;
constexpr unsigned WS_EPOCH = 0;
constexpr unsigned WS_Q = 4096;
constexpr unsigned WS_H = WS_Q + 8 * 16384;
constexpr unsigned WS_E1 = WS_H + 4 * 14336;
constexpr unsigned WS_E2 = WS_E1 + 256 * 4096;
constexpr unsigned WS_END = WS_E2 + 32 * 32768;
constexpr unsigned WS_STAMP = 16u << 20;
constexpr int SPIN_CAP = 1 << 18;
constexpr int AUX_NT = 2, AUX_SC1 = 16;

__device__ __forceinline__ float bflo(unsigned v) { return __uint_as_float(v << 16); }
__device__ __forceinline__ float bfhi(unsigned v) { return __uint_as_float(v & 0xffff0000u); }
__device__ __forceinline__ unsigned cvtpk(float lo, float hi) {
  bf16x2 r; r[0] = (__bf16)lo; r[1] = (__bf16)hi; return __builtin_bit_cast(unsigned, r);
}
__device__ __forceinline__ float rbf(float f) { return bflo(cvtpk(f, f)); }
template <int CTRL> __device__ __forceinline__ float dpp(float x) {
  return __builtin_bit_cast(float, __builtin_amdgcn_mov_dpp(__builtin_bit_cast(int, x), CTRL, 0xf, 0xf, true));
}
constexpr int ROR1 = 0x121, ROR2 = 0x122, ROR4 = 0x124, ROR8 = 0x128, SHL4 = 0x104, SHL8 = 0x108, SHL12 = 0x10c;
__device__ __forceinline__ float xrow_max(float x) {
  auto s = __builtin_amdgcn_permlane16_swap(__float_as_uint(x), __float_as_uint(x), false, false);
  x = fmaxf(__uint_as_float(s[0]), __uint_as_float(s[1]));
  auto t = __builtin_amdgcn_permlane32_swap(__float_as_uint(x), __float_as_uint(x), false, false);
  return fmaxf(__uint_as_float(t[0]), __uint_as_float(t[1]));
}
__device__ __forceinline__ float xrow_sum(float x) {
  auto s = __builtin_amdgcn_permlane16_swap(__float_as_uint(x), __float_as_uint(x), false, false);
  x = __uint_as_float(s[0]) + __uint_as_float(s[1]);
  auto t = __builtin_amdgcn_permlane32_swap(__float_as_uint(x), __float_as_uint(x), false, false);
  return __uint_as_float(t[0]) + __uint_as_float(t[1]);
}
__device__ __forceinline__ float row_sum(float x) {
  x += dpp<ROR8>(x); x += dpp<ROR4>(x); x += dpp<ROR2>(x); x += dpp<ROR1>(x); return x;
}
__device__ __forceinline__ float wave_sum(float x) { return xrow_sum(row_sum(x)); }
__device__ __forceinline__ float dot2(unsigned a, unsigned b, float c) {
  return __builtin_amdgcn_fdot2_f32_bf16(__builtin_bit_cast(bf16x2, a), __builtin_bit_cast(bf16x2, b), c, false);
}
__device__ __forceinline__ unsigned perm_lo(unsigned hi, unsigned lo) { return __builtin_amdgcn_perm(hi, lo, 0x05040100u); }
__device__ __forceinline__ unsigned perm_hi(unsigned hi, unsigned lo) { return __builtin_amdgcn_perm(hi, lo, 0x07060302u); }
__device__ __forceinline__ rsrc_t mkrsrc(const void* p, unsigned bytes) { return __builtin_amdgcn_make_buffer_rsrc(const_cast<void*>(p), 0, bytes, 0x00020000); }
__device__ __forceinline__ f32x4 mfma(u32x4 a, u32x4 b, f32x4 c) {
  return __builtin_amdgcn_mfma_f32_16x16x32_bf16(__builtin_bit_cast(bf16x8, a), __builtin_bit_cast(bf16x8, b), c, 0, 0, 0);
}

__device__ __forceinline__ void poll4(u32x4 (&a)[4], const char* base, unsigned off) {
  asm volatile("s_nop 1\n\tglobal_load_dwordx4 %0, %4, %5 sc1\n\tglobal_load_dwordx4 %1, %4, %5 offset:1024 sc1\n\t"
               "global_load_dwordx4 %2, %4, %5 offset:2048 sc1\n\tglobal_load_dwordx4 %3, %4, %5 offset:3072 sc1\n\ts_waitcnt vmcnt(0)"
               : "=&v"(a[0]), "=&v"(a[1]), "=&v"(a[2]), "=&v"(a[3]) : "v"(off), "s"(base) : "memory");
}
__device__ __forceinline__ void poll4m(u32x4 (&a)[4], const char* base, unsigned off, const u64 (&mk)[4]) {
  u64 sv;
  asm volatile("s_mov_b64 %4, exec\n\t"
               "s_mov_b64 exec, %7\n\ts_cbranch_execz 1f\n\tglobal_load_dwordx4 %0, %5, %6 sc1\n1:\n\t"
               "s_mov_b64 exec, %8\n\ts_cbranch_execz 2f\n\tglobal_load_dwordx4 %1, %5, %6 offset:1024 sc1\n2:\n\t"
               "s_mov_b64 exec, %9\n\ts_cbranch_execz 3f\n\tglobal_load_dwordx4 %2, %5, %6 offset:2048 sc1\n3:\n\t"
               "s_mov_b64 exec, %10\n\ts_cbranch_execz 4f\n\tglobal_load_dwordx4 %3, %5, %6 offset:3072 sc1\n4:\n\t"
               "s_mov_b64 exec, %4\n\ts_waitcnt vmcnt(0)"
               : "+v"(a[0]), "+v"(a[1]), "+v"(a[2]), "+v"(a[3]), "=&s"(sv)
               : "v"(off), "s"(base), "s"(mk[0]), "s"(mk[1]), "s"(mk[2]), "s"(mk[3]) : "memory");
}
__device__ __forceinline__ u32x4 poll1(const char* base, unsigned off) {
  u32x4 a;
  asm volatile("global_load_dwordx4 %0, %1, %2 sc1\n\ts_waitcnt vmcnt(0)" : "=&v"(a) : "v"(off), "s"(base) : "memory");
  return a;
}
__device__ __forceinline__ void poll32(u32x4 (&a)[8][4], const char* base, unsigned off, unsigned pend) {
  asm volatile("s_bitcmp1_b32 %18, 0\n\ts_cbranch_scc0 1f\n\t"
               "global_load_dwordx4 %0, %16, %17 offset:-4096 sc1\n\t"
               "global_load_dwordx4 %1, %16, %17 offset:-3072 sc1\n\t"
               "global_load_dwordx4 %2, %16, %17 offset:-2048 sc1\n\t"
               "global_load_dwordx4 %3, %16, %17 offset:-1024 sc1\n1:\n\t"
               "s_bitcmp1_b32 %18, 1\n\ts_cbranch_scc0 2f\n\t"
               "global_load_dwordx4 %4, %16, %17 offset:0 sc1\n\t"
               "global_load_dwordx4 %5, %16, %17 offset:1024 sc1\n\t"
               "global_load_dwordx4 %6, %16, %17 offset:2048 sc1\n\t"
               "global_load_dwordx4 %7, %16, %17 offset:3072 sc1\n2:\n\t"
               "s_bitcmp1_b32 %18, 2\n\ts_cbranch_scc0 3f\n\t"
               "global_load_dwordx4 %8, %19, %17 offset:-4096 sc1\n\t"
               "global_load_dwordx4 %9, %19, %17 offset:-3072 sc1\n\t"
               "global_load_dwordx4 %10, %19, %17 offset:-2048 sc1\n\t"
               "global_load_dwordx4 %11, %19, %17 offset:-1024 sc1\n3:\n\t"
               "s_bitcmp1_b32 %18, 3\n\ts_cbranch_scc0 4f\n\t"
               "global_load_dwordx4 %12, %19, %17 offset:0 sc1\n\t"
               "global_load_dwordx4 %13, %19, %17 offset:1024 sc1\n\t"
               "global_load_dwordx4 %14, %19, %17 offset:2048 sc1\n\t"
               "global_load_dwordx4 %15, %19, %17 offset:3072 sc1\n4:\n\t"
               "s_nop 0"
               : "+v"(a[0][0]), "+v"(a[0][1]), "+v"(a[0][2]), "+v"(a[0][3]), "+v"(a[1][0]), "+v"(a[1][1]), "+v"(a[1][2]), "+v"(a[1][3]), "+v"(a[2][0]), "+v"(a[2][1]), "+v"(a[2][2]), "+v"(a[2][3]), "+v"(a[3][0]), "+v"(a[3][1]), "+v"(a[3][2]), "+v"(a[3][3])
               : "v"(off + 4096), "s"(base), "s"(pend), "v"(off + 12288) : "memory", "scc");
  asm volatile("s_bitcmp1_b32 %18, 4\n\ts_cbranch_scc0 1f\n\t"
               "global_load_dwordx4 %0, %16, %17 offset:-4096 sc1\n\t"
               "global_load_dwordx4 %1, %16, %17 offset:-3072 sc1\n\t"
               "global_load_dwordx4 %2, %16, %17 offset:-2048 sc1\n\t"
               "global_load_dwordx4 %3, %16, %17 offset:-1024 sc1\n1:\n\t"
               "s_bitcmp1_b32 %18, 5\n\ts_cbranch_scc0 2f\n\t"
               "global_load_dwordx4 %4, %16, %17 offset:0 sc1\n\t"
               "global_load_dwordx4 %5, %16, %17 offset:1024 sc1\n\t"
               "global_load_dwordx4 %6, %16, %17 offset:2048 sc1\n\t"
               "global_load_dwordx4 %7, %16, %17 offset:3072 sc1\n2:\n\t"
               "s_bitcmp1_b32 %18, 6\n\ts_cbranch_scc0 3f\n\t"
               "global_load_dwordx4 %8, %19, %17 offset:-4096 sc1\n\t"
               "global_load_dwordx4 %9, %19, %17 offset:-3072 sc1\n\t"
               "global_load_dwordx4 %10, %19, %17 offset:-2048 sc1\n\t"
               "global_load_dwordx4 %11, %19, %17 offset:-1024 sc1\n3:\n\t"
               "s_bitcmp1_b32 %18, 7\n\ts_cbranch_scc0 4f\n\t"
               "global_load_dwordx4 %12, %19, %17 offset:0 sc1\n\t"
               "global_load_dwordx4 %13, %19, %17 offset:1024 sc1\n\t"
               "global_load_dwordx4 %14, %19, %17 offset:2048 sc1\n\t"
               "global_load_dwordx4 %15, %19, %17 offset:3072 sc1\n4:\n\t"
               "s_waitcnt vmcnt(0)"
               : "+v"(a[4][0]), "+v"(a[4][1]), "+v"(a[4][2]), "+v"(a[4][3]), "+v"(a[5][0]), "+v"(a[5][1]), "+v"(a[5][2]), "+v"(a[5][3]), "+v"(a[6][0]), "+v"(a[6][1]), "+v"(a[6][2]), "+v"(a[6][3]), "+v"(a[7][0]), "+v"(a[7][1]), "+v"(a[7][2]), "+v"(a[7][3])
               : "v"(off + 20480), "s"(base), "s"(pend), "v"(off + 28672) : "memory", "scc");
  asm volatile("" : "+v"(a[0][0]), "+v"(a[0][1]), "+v"(a[0][2]), "+v"(a[0][3]), "+v"(a[1][0]), "+v"(a[1][1]), "+v"(a[1][2]), "+v"(a[1][3]), "+v"(a[2][0]), "+v"(a[2][1]), "+v"(a[2][2]), "+v"(a[2][3]), "+v"(a[3][0]), "+v"(a[3][1]), "+v"(a[3][2]), "+v"(a[3][3]));
}
__device__ __forceinline__ unsigned umax4(const u32x4& a) { return max(max(a[0], a[1]), max(a[2], a[3])); }
constexpr unsigned EMPTY = 0xffffffffu;
#define SB() __builtin_amdgcn_sched_barrier(0)
#define PACE() do { SB(); asm volatile("s_waitcnt vmcnt(%0)" :: "i"(WIN - 1) : "memory"); } while (0)
#define BARRIER() do { SB(); asm volatile("s_waitcnt lgkmcnt(0)\n\ts_barrier" ::: "memory"); SB(); } while (0)
#define STAMP(i)
#define PROG(i)
#define STAMP_OUT()

__global__ void __launch_bounds__(512, 2) fused_decode(const u16* __restrict__ x, const u16* __restrict__ K, const u16* __restrict__ V,
                                                       const u16* __restrict__ wq, const u16* __restrict__ wo, const u16* __restrict__ w1,
                                                       const u16* __restrict__ w2, u16* __restrict__ out, char* ws) {
  __shared__ __attribute__((aligned(16))) float lds[2048 + 512 + 32 + 16 * 132 + 4];
  float* const sm_q = lds;
  float* const sm_y = lds + 2048;
  float* const sm_ml = lds + 2560;
  float* const sm_acc = lds + 2592;
  unsigned* const sm_tag = reinterpret_cast<unsigned*>(lds + 2592 + 16 * 132);
  const int b = blockIdx.x, g = b & 7, j = b >> 3;
  const int tid = threadIdx.x, lane = tid & 63, wave = __builtin_amdgcn_readfirstlane(tid >> 6);
  const int lg = lane >> 4, li = lane & 15;
  STAMP(0);
  asm volatile("" :: "s"(x), "s"(K), "s"(V), "s"(wq), "s"(wo), "s"(w1), "s"(w2), "s"(out), "s"(ws));

  if (__builtin_expect(wave < 4, 1)) {
    const rsrc_t rk = mkrsrc((const char*)K + (size_t)g * HEADB, HEADB), rv = mkrsrc((const char*)V + (size_t)g * HEADB, HEADB);
    const unsigned koff = (li >> 2) * 256 + lg * 64 + (li & 3) * 16, voff = lane * 16;
    const unsigned wbase = j * 131072u + wave * 8192u;
    auto toff = [&](int n) -> unsigned { return wbase + (unsigned)(n >> 2) * 4194304u + (unsigned)(n & 3) * 32768u; };
    auto ldk = [&](unsigned so, int t) -> u32x4 { return __builtin_amdgcn_raw_buffer_load_b128(rk, koff + (t & 3) * 1024, so + (t >> 2) * 4096, AUX_NT); };
    auto ldv = [&](unsigned so, int t) -> u32x4 { return __builtin_amdgcn_raw_buffer_load_b128(rv, voff + (t & 3) * 1024, so + (t >> 2) * 4096, AUX_NT); };
    u32x4 kb[2][8], vb[2][8];
#pragma unroll
    for (int u = 0; u < 2; ++u) {
#pragma unroll
      for (int t = 0; t < 8; ++t) { if (u * 16 + t >= WIN) PACE(); kb[u][t] = ldk(toff(u), t); SB(); }
      if (u == 0) STAMP(1);
#pragma unroll
      for (int t = 0; t < 8; ++t) { if (u * 16 + 8 + t >= WIN) PACE(); vb[u][t] = ldv(toff(u), t); SB(); }
    }
    STAMP(2);
    BARRIER();
    STAMP(3);
    u32x4 qb;
    {
      const float* qp = sm_q + (li & 3) * 128 + 8 * (4 * lg + (li >> 2));
      f32x4 a = *reinterpret_cast<const f32x4*>(qp), c = *reinterpret_cast<const f32x4*>(qp + 4);
#pragma unroll
      for (int r = 1; r < 4; ++r) { a += *reinterpret_cast<const f32x4*>(qp + 512 * r); c += *reinterpret_cast<const f32x4*>(qp + 512 * r + 4); }
      qb = u32x4{cvtpk(a[0], a[1]), cvtpk(a[2], a[3]), cvtpk(c[0], c[1]), cvtpk(c[2], c[3])};
    }
    f32x4 acc[8], accl = {0, 0, 0, 0};
#pragma unroll
    for (int e = 0; e < 8; ++e) acc[e] = f32x4{0, 0, 0, 0};
    float m = -INFINITY, s[8];
    const u32x4 ones = {0x3f803f80u, 0x3f803f80u, 0x3f803f80u, 0x3f803f80u};
    auto qk = [&](const u32x4& kk) -> float {
      const f32x4 d = mfma(kk, qb, f32x4{0, 0, 0, 0});
      float v = d[0] + dpp<SHL4>(d[1]);
      v += dpp<SHL8>(d[2]);
      v += dpp<SHL12>(d[3]);
      return rbf(rbf(v) * 0.088388347648318440f);
    };
#pragma unroll
    for (int t = 0; t < 8; ++t) s[t] = qk(kb[0][t]);
    auto tile = [&](u32x4 (&kc)[8], u32x4 (&vc)[8], const u32x4 (&kn)[8], unsigned so, auto refill, auto next) {
      constexpr bool R = decltype(refill)::value, N = decltype(next)::value;
      constexpr float L2E = 1.4426950408889634f;
#define KSTEP(t) do { if constexpr (R) { PACE(); kc[t] = ldk(so, t); SB(); } } while (0)
      KSTEP(0);
      float tm = fmaxf(fmaxf(fmaxf(s[0], s[1]), fmaxf(s[2], s[3])), fmaxf(fmaxf(s[4], s[5]), fmaxf(s[6], s[7])));
      tm = xrow_max(tm);
      const float mn = fmaxf(m, tm);
      const float alpha = __builtin_amdgcn_exp2f((m - mn) * L2E);
      m = mn;
      KSTEP(1);
      u32x4 pb;
#pragma unroll
      for (int k = 0; k < 4; ++k) pb[k] = cvtpk(__builtin_amdgcn_exp2f((s[2 * k] - mn) * L2E), __builtin_amdgcn_exp2f((s[2 * k + 1] - mn) * L2E));
      KSTEP(2);
#pragma unroll
      for (int e = 0; e < 8; ++e) acc[e] *= alpha;
      accl *= alpha;
      KSTEP(3);
      accl = mfma(ones, pb, accl);
#pragma unroll
      for (int e = 0; e < 8; ++e) {
        u32x4 f;
#pragma unroll
        for (int k = 0; k < 4; ++k) f[k] = (e & 1) ? perm_hi(vc[2 * k + 1][e >> 1], vc[2 * k][e >> 1]) : perm_lo(vc[2 * k + 1][e >> 1], vc[2 * k][e >> 1]);
        acc[e] = mfma(f, pb, acc[e]);
        if (e & 1) KSTEP(4 + (e >> 1));
      }
#undef KSTEP
#pragma unroll
      for (int t = 0; t < 8; ++t) {
        if constexpr (R) { PACE(); vc[t] = ldv(so, t); SB(); }
        if constexpr (N) s[t] = qk(kn[t]);
      }
    };
    using T = std::integral_constant<bool, true>;
    using F = std::integral_constant<bool, false>;
#pragma unroll 1
    for (int it = 0; it < NT - 2; it += 2) {
      tile(kb[0], vb[0], kb[1], toff(it + 2), T{}, T{});
      PROG(it);
      tile(kb[1], vb[1], kb[0], toff(it + 3), T{}, T{});
      PROG(it + 1);
    }
    STAMP(4);
    tile(kb[0], vb[0], kb[1], 0u, F{}, T{});
    tile(kb[1], vb[1], kb[0], 0u, F{}, F{});
    STAMP(5);
    if (li < 4) {
      float* a = sm_acc + (wave * 4 + li) * 132 + 32 * lg;
#pragma unroll
      for (int r = 0; r < 4; ++r) {
        *reinterpret_cast<f32x4*>(a + 8 * r) = f32x4{acc[0][r], acc[1][r], acc[2][r], acc[3][r]};
        *reinterpret_cast<f32x4*>(a + 8 * r + 4) = f32x4{acc[4][r], acc[5][r], acc[6][r], acc[7][r]};
      }
      if (lg == 0) *reinterpret_cast<f32x2*>(sm_ml + (wave * 4 + li) * 2) = f32x2{m, accl[0]};
    }
    BARRIER();
    STAMP(6);
    if (wave == 0 && j < 4) {
      const int f = j * 8 + g;
      const char* ib = ws + WS_E2 + f * 32768;
      const unsigned xo = *reinterpret_cast<const unsigned*>(x + 16 * f + 2 * (lane & 7));
      const unsigned tag = __builtin_amdgcn_readfirstlane(*sm_tag);
      u32x4 py[8][4];
      unsigned pend = 0xffu;
#pragma unroll 1
      for (int spin = 0; spin < SPIN_CAP; ++spin) {
        poll32(py, ib, lane * 16, pend);
#pragma unroll
        for (int i = 0; i < 8; ++i) {
          if (pend & (1u << i)) {
            unsigned bad = 0;
#pragma unroll
            for (int k = 0; k < 4; ++k) bad |= (py[i][k][1] ^ tag) | (py[i][k][3] ^ tag);
            if (__builtin_amdgcn_ballot_w64(bad != 0) == 0) {
              pend &= ~(1u << i);
              py[i][0][0] = __float_as_uint((__uint_as_float(py[i][0][0]) + __uint_as_float(py[i][1][0])) + (__uint_as_float(py[i][2][0]) + __uint_as_float(py[i][3][0])));
              py[i][0][2] = __float_as_uint((__uint_as_float(py[i][0][2]) + __uint_as_float(py[i][1][2])) + (__uint_as_float(py[i][2][2]) + __uint_as_float(py[i][3][2])));
            }
          }
        }
        if (!pend) break;
        __builtin_amdgcn_s_sleep(E2SLEEP);
      }
      STAMP(9);
      float s0 = 0.f, s1 = 0.f;
#pragma unroll
      for (int i = 0; i < 8; ++i) { s0 += __uint_as_float(py[i][0][0]); s1 += __uint_as_float(py[i][0][2]); }
      s0 = xrow_sum(s0 + dpp<ROR8>(s0)); s1 = xrow_sum(s1 + dpp<ROR8>(s1));
      if (lane < 8) *reinterpret_cast<unsigned*>(out + 16 * f + 2 * lane) = cvtpk(bflo(xo) + s0, bfhi(xo) + s1);
      if (b == 0 && lane == 0) __hip_atomic_store(reinterpret_cast<unsigned*>(ws + WS_EPOCH), tag, __ATOMIC_RELAXED, __HIP_MEMORY_SCOPE_AGENT);
      STAMP(10);
    }
    STAMP_OUT();
    return;
  }

  const int hw = wave - 4;
  const rsrc_t rw = mkrsrc(ws, WS_END);
  const unsigned epoch = __builtin_amdgcn_raw_buffer_load_b32(rw, 0, WS_EPOCH, AUX_SC1);
  unsigned tag;
  auto ln_stats = [&](const u32x4& xr, float& mb, float& rden) {
    float xf[8];
#pragma unroll
    for (int k = 0; k < 4; ++k) { xf[2 * k] = bflo(xr[k]); xf[2 * k + 1] = bfhi(xr[k]); }
    const float mean = wave_sum(((xf[0] + xf[1]) + (xf[2] + xf[3])) + ((xf[4] + xf[5]) + (xf[6] + xf[7]))) * (1.f / 512.f);
    float vs = 0.f;
#pragma unroll
    for (int k = 0; k < 8; ++k) { const float d = xf[k] - mean; vs += d * d; }
    const float var = wave_sum(vs) * (1.f / 512.f);
    mb = rbf(mean); rden = __builtin_amdgcn_rcpf(rbf(__builtin_amdgcn_sqrtf(rbf(rbf(var) + 1e-5f))));
  };
  auto ln2 = [&](unsigned xp, float mb, float rden) -> unsigned { return cvtpk(rbf(bflo(xp) - mb) * rden, rbf(bfhi(xp) - mb) * rden); };
  auto fold8 = [&](const float (&a)[8], float (&c2)[2]) {
    float b4[4];
#pragma unroll
    for (int k = 0; k < 4; ++k) { auto r = __builtin_amdgcn_permlane32_swap(__float_as_uint(a[k]), __float_as_uint(a[k + 4]), false, false); b4[k] = __uint_as_float(r[0]) + __uint_as_float(r[1]); }
#pragma unroll
    for (int k = 0; k < 2; ++k) { auto r = __builtin_amdgcn_permlane16_swap(__float_as_uint(b4[k]), __float_as_uint(b4[k + 2]), false, false); c2[k] = __uint_as_float(r[0]) + __uint_as_float(r[1]); }
  };
  if (hw < 2) {
    const int item = g * 14 + j, r8 = lane >> 3, c8 = lane & 7;
    const bool on = hw == 0 || j < 14;
    const int rb = hw == 0 ? j >> 3 : item & 3;
    const unsigned ldb = hw == 0 ? 8192u : (unsigned)DFF * 2, colb = hw == 0 ? g * 1024 + (j & 7) * 128 : (item >> 2) * 128;
    const unsigned dst = hw == 0 ? WS_Q + g * 16384 + rb * 4096 + (j & 7) * 512 : WS_H + rb * 14336 + (item >> 2) * 512;
    const u32x4 xr = *reinterpret_cast<const u32x4*>(x + 8 * lane);
    const u32x4 xa = *reinterpret_cast<const u32x4*>(x + 128 * rb + 16 * r8), xb = *reinterpret_cast<const u32x4*>(x + 128 * rb + 16 * r8 + 8);
    const rsrc_t rm = mkrsrc(hw == 0 ? wq : w1, on ? DM * ldb : 0);
    u32x4 wr[16];
#pragma unroll
    for (int i = 0; i < 16; ++i) wr[i] = __builtin_amdgcn_raw_buffer_load_b128(rm, r8 * 16 * ldb + c8 * 16, (128 * rb + i) * ldb + colb, AUX_NT);
    SB();
    tag = __builtin_amdgcn_readfirstlane(epoch) + 1u;
    float mb, rden;
    ln_stats(xr, mb, rden);
    unsigned xn[8];
#pragma unroll
    for (int k = 0; k < 4; ++k) { xn[k] = ln2(xa[k], mb, rden); xn[4 + k] = ln2(xb[k], mb, rden); }
    STAMP(1);
    float a[8] = {0, 0, 0, 0, 0, 0, 0, 0};
#pragma unroll
    for (int k = 0; k < 8; ++k)
#pragma unroll
      for (int c = 0; c < 8; ++c) a[c] = dot2((c & 1) ? perm_hi(wr[2 * k + 1][c >> 1], wr[2 * k][c >> 1]) : perm_lo(wr[2 * k + 1][c >> 1], wr[2 * k][c >> 1]), xn[k], a[c]);
    float c2[2];
    fold8(a, c2);
    c2[0] += dpp<ROR8>(c2[0]); c2[1] += dpp<ROR8>(c2[1]);
    if (li < 8 && on) __builtin_amdgcn_raw_buffer_store_b128(u32x4{__float_as_uint(c2[0]), tag, __float_as_uint(c2[1]), tag}, rw, lg * 128 + c8 * 16, dst, AUX_SC1);
    if (hw == 0 && lane == 0) *(volatile LAS unsigned*)sm_tag = tag;
    STAMP(2);
  } else {
    tag = __builtin_amdgcn_readfirstlane(epoch) + 1u;
#pragma unroll 1
    for (int spin = 0; spin < 4096 && __builtin_amdgcn_readfirstlane(*(volatile LAS unsigned*)sm_tag) != tag; ++spin) __builtin_amdgcn_s_sleep(1);
  }
  __builtin_amdgcn_s_sleep(QSLEEP);
  {
    u32x4 pq[4];
    poll4(pq, ws + WS_Q + g * 16384 + hw * 4096, lane * 16);
#pragma unroll 1
    for (int spin = 0; spin < SPIN_CAP; ++spin) {
      u64 mk[4];
#pragma unroll
      for (int i = 0; i < 4; ++i) mk[i] = __builtin_amdgcn_ballot_w64(((pq[i][1] ^ tag) | (pq[i][3] ^ tag)) != 0);
      if (!(mk[0] | mk[1] | mk[2] | mk[3])) break;
      __builtin_amdgcn_s_sleep(2);
      poll4m(pq, ws + WS_Q + g * 16384 + hw * 4096, lane * 16, mk);
    }
#pragma unroll
    for (int i = 0; i < 4; ++i) *reinterpret_cast<u32x2*>(sm_q + hw * 512 + i * 128 + (lane >> 5) * 64 + (lane & 7) * 8 + ((lane >> 3) & 3) * 2) = u32x2{pq[i][0], pq[i][2]};
  }
  STAMP(11);
  BARRIER();
  STAMP(3);
  if (hw == 1) {
    const bool has_ffn = j < FFNJ;
    const int ub = has_ffn ? g * 28 + j : 0;
    const rsrc_t r2 = mkrsrc(w2, has_ffn ? DFF * DM * 2 : 0);
    __builtin_amdgcn_s_sleep(64);
    u32x4 w2r[8];
#pragma unroll
    for (int i = 0; i < 8; ++i) w2r[i] = __builtin_amdgcn_raw_buffer_load_b128(r2, lane * 16, (ub * 8 + i) * 1024, AUX_NT);
    u32x4 ph;
#pragma unroll 1
    for (int spin = 0; spin < SPIN_CAP; ++spin) {
      ph = poll1(ws, WS_H + ((li >> 2) * 14336 + (ub >> 3) * 512 + (li & 3) * 128 + (ub & 7) * 16));
      if (__builtin_amdgcn_ballot_w64(((ph[1] ^ tag) | (ph[3] ^ tag)) != 0) == 0) break;
      __builtin_amdgcn_s_sleep(32);
    }
    float h0 = __uint_as_float(ph[0]), h1 = __uint_as_float(ph[2]);
    h0 += dpp<ROR8>(h0); h1 += dpp<ROR8>(h1); h0 += dpp<ROR4>(h0); h1 += dpp<ROR4>(h1);
    const unsigned hp = cvtpk(fmaxf(h0, 0.f), fmaxf(h1, 0.f));
    float y[8] = {0, 0, 0, 0, 0, 0, 0, 0};
#pragma unroll
    for (int k = 0; k < 4; ++k) {
      const unsigned hk = __builtin_amdgcn_readlane(hp, k);
#pragma unroll
      for (int c = 0; c < 8; ++c) y[c] = dot2((c & 1) ? perm_hi(w2r[2 * k + 1][c >> 1], w2r[2 * k][c >> 1]) : perm_lo(w2r[2 * k + 1][c >> 1], w2r[2 * k][c >> 1]), hk, y[c]);
    }
    *reinterpret_cast<f32x4*>(sm_y + 8 * lane) = f32x4{y[0], y[1], y[2], y[3]};
    *reinterpret_cast<f32x4*>(sm_y + 8 * lane + 4) = f32x4{y[4], y[5], y[6], y[7]};
  }
  unsigned wop[16];
  {
    const rsrc_t ro = mkrsrc(wo, 4096 * DM * 2);
    unsigned a[16];
#pragma unroll
    for (int i = 0; i < 16; ++i) a[i] = __builtin_amdgcn_raw_buffer_load_b32(ro, lane * 4, (g * 512 + j * 16 + i) * 1024 + hw * 256, AUX_NT);
#pragma unroll
    for (int k = 0; k < 8; ++k) { wop[2 * k] = perm_lo(a[2 * k + 1], a[2 * k]); wop[2 * k + 1] = perm_hi(a[2 * k + 1], a[2 * k]); }
  }
#pragma unroll
  for (int k = 0; k < 16; ++k) asm volatile("" : "+v"(wop[k]));
  PROG(0);
  constexpr float L2E = 1.4426950408889634f;
  const int pidx = g * 32 + j;
  const unsigned tag16 = tag & 0xffffu;
#pragma unroll 1
  for (int pass = DRY ? 0 : 1; pass < 2; ++pass) {
    const rsrc_t rs = mkrsrc(ws, pass ? WS_END : 0);
    if (pass) {
      STAMP(4);
      BARRIER();
      STAMP(5);
    }
    f32x2 yy;
    {
      f32x2 ml[4], av[4];
#pragma unroll
      for (int w = 0; w < 4; ++w) { ml[w] = *reinterpret_cast<const f32x2*>(sm_ml + (w * 4 + hw) * 2); av[w] = *reinterpret_cast<const f32x2*>(sm_acc + (w * 4 + hw) * 132 + 2 * lane); }
      yy = *reinterpret_cast<const f32x2*>(sm_y + 128 * hw + 2 * lane);
      const float M = fmaxf(fmaxf(ml[0][0], ml[1][0]), fmaxf(ml[2][0], ml[3][0]));
      float L = 0.f, o0 = 0.f, o1 = 0.f;
#pragma unroll
      for (int w = 0; w < 4; ++w) { const float wt = __builtin_amdgcn_exp2f((ml[w][0] - M) * L2E); L += wt * ml[w][1]; o0 += wt * av[w][0]; o1 += wt * av[w][1]; }
      const float rl = __builtin_amdgcn_rcpf(L);
      const unsigned lse = __float_as_uint(M * L2E + __builtin_amdgcn_logf(L));
      __builtin_amdgcn_raw_buffer_store_b128(u32x4{__float_as_uint(o0 * rl), (lse & 0xffff0000u) | tag16, __float_as_uint(o1 * rl), (lse << 16) | tag16}, rs,
                                             (lane >> 3) * 4096 + (lane & 7) * 16, WS_E1 + (g * 32 + hw * 8) * 4096 + j * 128, AUX_SC1);
    }
    STAMP(6);
    u32x4 pa[4];
#pragma unroll
    for (int i = 0; i < 4; ++i) asm volatile("" : "=v"(pa[i]));
    u64 mk[4];
#pragma unroll
    for (int i = 0; i < 4; ++i) mk[i] = pass ? ~0ull : 0ull;
#pragma unroll 1
    for (int spin = 0; spin < SPIN_CAP; ++spin) {
      poll4m(pa, ws + WS_E1 + pidx * 4096, lane * 16, mk);
#pragma unroll
      for (int i = 0; i < 4; ++i) {
        u64 m8 = __builtin_amdgcn_ballot_w64((((pa[i][1] ^ tag16) | (pa[i][3] ^ tag16)) & 0xffffu) != 0) & mk[i];
        m8 |= m8 >> 4; m8 |= m8 >> 2; m8 |= m8 >> 1;
        mk[i] = (m8 & 0x0101010101010101ull) * 0xffull;
      }
      if (!(mk[0] | mk[1] | mk[2] | mk[3])) break;
      __builtin_amdgcn_s_sleep(E1SLEEP);
    }
    STAMP(7);
    float ls[4];
#pragma unroll
    for (int i = 0; i < 4; ++i) ls[i] = __uint_as_float(perm_hi(pa[i][1], pa[i][3]));
    float M = fmaxf(fmaxf(ls[0], ls[1]), fmaxf(ls[2], ls[3]));
    M = xrow_max(fmaxf(M, dpp<ROR8>(M)));
    float L = 0.f, o0 = 0.f, o1 = 0.f;
#pragma unroll
    for (int i = 0; i < 4; ++i) { const float wt = __builtin_amdgcn_exp2f(ls[i] - M); L += wt; o0 += wt * __uint_as_float(pa[i][0]); o1 += wt * __uint_as_float(pa[i][2]); }
    L = xrow_sum(L + dpp<ROR8>(L)); o0 = xrow_sum(o0 + dpp<ROR8>(o0)); o1 = xrow_sum(o1 + dpp<ROR8>(o1));
    const float rl = __builtin_amdgcn_rcpf(L);
    const unsigned op = cvtpk(o0 * rl, o1 * rl);
    float y0 = yy[0], y1 = yy[1];
#pragma unroll
    for (int k = 0; k < 8; ++k) { const unsigned ok2 = __builtin_amdgcn_readlane(op, k); y0 = dot2(wop[2 * k], ok2, y0); y1 = dot2(wop[2 * k + 1], ok2, y1); }
    __builtin_amdgcn_raw_buffer_store_b128(u32x4{__float_as_uint(y0), tag, __float_as_uint(y1), tag}, rs, (lane >> 3) * 32768 + (lane & 7) * 16, WS_E2 + hw * 8 * 32768 + pidx * 128, AUX_SC1);
    STAMP(8);
  }
  STAMP_OUT();
}

extern "C" void kernel_launch(void* const* d_in, const int* in_sizes, int n_in, void* d_out, int out_size, void* d_ws, size_t ws_size,
                              hipStream_t stream) {
  fused_decode<<<256, 512, 0, stream>>>((const u16*)d_in[0], (const u16*)d_in[1], (const u16*)d_in[2], (const u16*)d_in[3], (const u16*)d_in[4],
                                        (const u16*)d_in[6], (const u16*)d_in[7], (u16*)d_out, (char*)d_ws);
}
```

```cpp
#include <hip/hip_runtime.h>
#include <hip/hip_bf16.h>
#include <stdint.h>

#define WIN 6
#define WIN0 WIN
#define QSLEEP 20
#define DRY 1
#define LATEW 1
#define THR1 0
#define SLP1 0
#define CTRL 1
#define CP 218
#define CT0 560
#define CG 8
#define CW0 10
#define CD 2
#define CMIN 8
#define CMAX 16
#define QSTR 16384
#define SDELAY 12
#define QPOS 31
#define PREWARM 3
#define PREG 0
#define ABL 0
#define WAUX AUX_NT
#define CODEPF 1
#define CODEPF_BYTES 17664
#define STAMPS 0

typedef unsigned short u16;
typedef unsigned long long u64;
typedef unsigned u32x4 __attribute__((ext_vector_type(4)));
typedef unsigned u32x2 __attribute__((ext_vector_type(2)));
typedef float f32x4 __attribute__((ext_vector_type(4)));
typedef float f32x2 __attribute__((ext_vector_type(2)));
typedef __bf16 bf16x8 __attribute__((ext_vector_type(8)));
typedef __bf16 bf16x2 __attribute__((ext_vector_type(2)));
typedef u64 u64x8 __attribute__((ext_vector_type(8)));
typedef __amdgpu_buffer_rsrc_t rsrc_t;
#define LAS __attribute__((address_space(3)))

constexpr int DM = 512, SK = 65536, DH = 128, DFF = 1792;
constexpr int NT = 16;
constexpr unsigned HEADB = SK * DH * 2;
constexpr unsigned WS_EPOCH = 0;
constexpr unsigned WS_Q = 4096;
constexpr unsigned WS_H = WS_Q + 8 * QSTR;
constexpr unsigned WS_E1 = WS_H + 4 * 14336;
constexpr unsigned WS_E2 = WS_E1 + 256 * 4096;
constexpr unsigned WS_T = WS_E2 + 32 * 16384;
constexpr unsigned WS_END = WS_T + 16 * 1024;
constexpr unsigned WS_STAMP = 16u << 20;
constexpr int SPIN_CAP = 1 << 18;
constexpr int AUX_NT = 2, AUX_SC1 = 16;
constexpr float L2E = 1.4426950408889634f, SCALE = 0.088388347648318440f;

__device__ __forceinline__ float bflo(unsigned v) { return __uint_as_float(v << 16); }
__device__ __forceinline__ float bfhi(unsigned v) { return __uint_as_float(v & 0xffff0000u); }
__device__ __forceinline__ unsigned cvtpk(float lo, float hi) {
  bf16x2 r; r[0] = (__bf16)lo; r[1] = (__bf16)hi; return __builtin_bit_cast(unsigned, r);
}
__device__ __forceinline__ float rbf(float f) { return bflo(cvtpk(f, f)); }
template <int DC> __device__ __forceinline__ float dpp(float x) {
  return __builtin_bit_cast(float, __builtin_amdgcn_mov_dpp(__builtin_bit_cast(int, x), DC, 0xf, 0xf, true));
}
constexpr int ROR1 = 0x121, ROR2 = 0x122, ROR4 = 0x124, ROR8 = 0x128, SHL4 = 0x104, SHL8 = 0x108, SHL12 = 0x10c;
__device__ __forceinline__ float xrow_max(float x) {
  auto s = __builtin_amdgcn_permlane16_swap(__float_as_uint(x), __float_as_uint(x), false, false);
  x = fmaxf(__uint_as_float(s[0]), __uint_as_float(s[1]));
  auto t = __builtin_amdgcn_permlane32_swap(__float_as_uint(x), __float_as_uint(x), false, false);
  return fmaxf(__uint_as_float(t[0]), __uint_as_float(t[1]));
}
__device__ __forceinline__ float xrow_sum(float x) {
  auto s = __builtin_amdgcn_permlane16_swap(__float_as_uint(x), __float_as_uint(x), false, false);
  x = __uint_as_float(s[0]) + __uint_as_float(s[1]);
  auto t = __builtin_amdgcn_permlane32_swap(__float_as_uint(x), __float_as_uint(x), false, false);
  return __uint_as_float(t[0]) + __uint_as_float(t[1]);
}
__device__ __forceinline__ float row_sum(float x) {
  x += dpp<ROR8>(x); x += dpp<ROR4>(x); x += dpp<ROR2>(x); x += dpp<ROR1>(x); return x;
}
__device__ __forceinline__ float wave_sum(float x) { return xrow_sum(row_sum(x)); }
__device__ __forceinline__ float dot2(unsigned a, unsigned b, float c) {
  return __builtin_amdgcn_fdot2_f32_bf16(__builtin_bit_cast(bf16x2, a), __builtin_bit_cast(bf16x2, b), c, false);
}
__device__ __forceinline__ unsigned perm_lo(unsigned hi, unsigned lo) { return __builtin_amdgcn_perm(hi, lo, 0x05040100u); }
__device__ __forceinline__ unsigned perm_hi(unsigned hi, unsigned lo) { return __builtin_amdgcn_perm(hi, lo, 0x07060302u); }
__device__ __forceinline__ unsigned colpair(const u32x4& r0, const u32x4& r1, int c) {
  return (c & 1) ? perm_hi(r1[c >> 1], r0[c >> 1]) : perm_lo(r1[c >> 1], r0[c >> 1]);
}
__device__ __forceinline__ rsrc_t mkrsrc(const void* p, unsigned bytes) { return __builtin_amdgcn_make_buffer_rsrc(const_cast<void*>(p), 0, bytes, 0x00020000); }
__device__ __forceinline__ f32x4 mfma(u32x4 a, u32x4 b, f32x4 c) {
  return __builtin_amdgcn_mfma_f32_16x16x32_bf16(__builtin_bit_cast(bf16x8, a), __builtin_bit_cast(bf16x8, b), c, 0, 0, 0);
}
__device__ __forceinline__ void fold8(const float (&a)[8], float (&c2)[2]) {
  float b4[4];
#pragma unroll
  for (int k = 0; k < 4; ++k) { auto r = __builtin_amdgcn_permlane32_swap(__float_as_uint(a[k]), __float_as_uint(a[k + 4]), false, false); b4[k] = __uint_as_float(r[0]) + __uint_as_float(r[1]); }
#pragma unroll
  for (int k = 0; k < 2; ++k) { auto r = __builtin_amdgcn_permlane16_swap(__float_as_uint(b4[k]), __float_as_uint(b4[k + 2]), false, false); c2[k] = __uint_as_float(r[0]) + __uint_as_float(r[1]); }
}

#define GATHER_CHECK \
  "v_bitop3_b32 v56, v41, v43, %[tag] bitop3:0x7e\n\tv_bitop3_b32 v57, v45, v47, %[tag] bitop3:0x7e\n\t" \
  "v_bitop3_b32 v58, v49, v51, %[tag] bitop3:0x7e\n\tv_bitop3_b32 v59, v53, v55, %[tag] bitop3:0x7e\n\t" \
  "v_cmp_ne_u16 %[m0], 0, v56\n\tv_cmp_ne_u16 %[m1], 0, v57\n\tv_cmp_ne_u16 %[m2], 0, v58\n\tv_cmp_ne_u16 %[m3], 0, v59\n\t" \
  "s_or_b64 %[t0], %[m0], %[m1]\n\ts_or_b64 vcc, %[m2], %[m3]\n\ts_or_b64 %[t0], %[t0], vcc\n\t"
#define GATHER_LOADS \
  "s_wqm_b64 exec, %[m0]\n\tglobal_load_dwordx4 v[40:43], %[off], %[base] sc1\n\t" \
  "s_wqm_b64 exec, %[m1]\n\tglobal_load_dwordx4 v[44:47], %[off], %[base] offset:1024 sc1\n\t" \
  "s_wqm_b64 exec, %[m2]\n\tglobal_load_dwordx4 v[48:51], %[off], %[base] offset:2048 sc1\n\t" \
  "s_wqm_b64 exec, %[m3]\n\tglobal_load_dwordx4 v[52:55], %[off], %[base] offset:3072 sc1\n\t" \
  "s_mov_b64 exec, -1\n\t"
template <int THR, int SLP, bool BAR>
__device__ __forceinline__ void gather4(u32x4 (&a)[4], const char* base, unsigned off, unsigned tag, unsigned init, int bar) {
  u64 m0, m1, m2, m3, t0; int cnt = SPIN_CAP;
  asm volatile(
      "s_nop 4\n\tv_mov_b32 v41, %[init]\n\tv_mov_b32 v43, %[init]\n\tv_mov_b32 v45, %[init]\n\tv_mov_b32 v47, %[init]\n\t"
      "v_mov_b32 v49, %[init]\n\tv_mov_b32 v51, %[init]\n\tv_mov_b32 v53, %[init]\n\tv_mov_b32 v55, %[init]\n\t"
      ".if %c[hasbar]\n\t"
      "2:\n\ts_cmp_eq_u32 %[bar], 0\n\ts_cbranch_scc1 0f\n\ts_barrier\n\t"
      ".endif\n\t"
      "0:\n\t" GATHER_CHECK
      "s_cbranch_scc0 9f\n\t" GATHER_LOADS
      "s_waitcnt vmcnt(%c[thr])\n\t"
      ".if %c[slp]\n\ts_sleep %c[slp]\n\t.endif\n\t"
      "s_sub_u32 %[cnt], %[cnt], 1\n\ts_cbranch_scc0 0b\n\t"
      "9:\n\ts_waitcnt vmcnt(0)"
      : "=&{v[40:43]}"(a[0]), "=&{v[44:47]}"(a[1]), "=&{v[48:51]}"(a[2]), "=&{v[52:55]}"(a[3]),
        [m0] "=&s"(m0), [m1] "=&s"(m1), [m2] "=&s"(m2), [m3] "=&s"(m3), [t0] "=&s"(t0), [cnt] "+s"(cnt)
      : [tag] "s"(tag), [init] "s"(init), [base] "s"(base), [off] "v"(off), [bar] "s"(bar), [thr] "i"(THR), [slp] "i"(SLP), [hasbar] "i"(BAR ? 1 : 0)
      : "memory", "vcc", "scc", "v56", "v57", "v58", "v59");
}
__device__ __forceinline__ u32x4 poll1(const char* base, unsigned off) {
  u32x4 a;
  asm volatile("global_load_dwordx4 %0, %1, %2 sc1\n\ts_waitcnt vmcnt(0)" : "=&v"(a) : "v"(off), "s"(base) : "memory");
  return a;
}

#define SB() __builtin_amdgcn_sched_barrier(0)
#define PACE() do { SB(); asm volatile("s_waitcnt vmcnt(%0)" :: "i"(WIN - 1) : "memory"); } while (0)
#define PACEV(t) do { SB(); asm volatile("s_swappc_b64 s[96:97], %0" :: "s"(((t) & 1) ? tgtB : tgtA) : "memory", "s96", "s97"); } while (0)
#define VMDONE() __builtin_amdgcn_s_waitcnt(0x0f70)
#define BARRIER() do { SB(); asm volatile("s_waitcnt lgkmcnt(0)\n\ts_barrier" ::: "memory"); SB(); } while (0)
#define STAMP(i)
#define PROG(i)
#define STAMP_OUT()

__global__ void __launch_bounds__(512, 2) __attribute__((aligned(4096)))
fused_decode(const void* K_, const void* V_, void* ws_, const void* x_, const void* wq_, const void* w1_, const void* wo_, const void* w2_, void* out_) {
  __shared__ __attribute__((aligned(16))) float lds[2048 + 32 + 16 * 132 + 64 + 4 + 4 * NT * 256];
  float* const sm_q = lds;
  float* const sm_ml = lds + 2048;
  float* const sm_acc = lds + 2080;
  float* const sm_o = lds + 2080 + 16 * 132;
  unsigned* const sm_tag = reinterpret_cast<unsigned*>(sm_o + 64);
  float* const sm_s = sm_o + 68;
  const int b = blockIdx.x, g = b & 7, j = b >> 3;
  const int tid = threadIdx.x, lane = tid & 63, wave = __builtin_amdgcn_readfirstlane(tid >> 6);
  const int lg = lane >> 4, li = lane & 15;
  STAMP(0);
  const unsigned tstart = (unsigned)__builtin_amdgcn_s_memrealtime();
  unsigned koff = (li >> 2) * 256 + lg * 64 + (li & 3) * 16, voff = lane * 16, wbase = j * 131072u + wave * 8192u;
  u64x8 ka; u64 ka8;
  {
    unsigned dummy; u64 pc;
    asm volatile("s_load_dwordx16 %0, %4, 0\n\ts_load_dwordx2 %1, %4, 0x40\n\t"
                 "s_cmp_lg_u32 %6, %c7\n\ts_cbranch_scc1 1f\n\ts_getpc_b64 %3\n\ts_lshr_b64 %3, %3, 12\n\ts_lshl_b64 %3, %3, 12\n\tglobal_load_dword %2, %5, %3\n\ts_waitcnt vmcnt(0)\n1:\n\t"
                 "s_waitcnt lgkmcnt(0)"
                 : "=&s"(ka), "=&s"(ka8), "=&v"(dummy), "=&s"(pc)
                 : "s"(__builtin_amdgcn_kernarg_segment_ptr()), "v"(min(j * 1024 + (lane & 7) * 128, CODEPF_BYTES - 4)), "s"(wave), "i"(CODEPF ? 7 : 99) : "memory", "scc");
  }
  const char *K = (const char*)ka[0], *V = (const char*)ka[1], *x = (const char*)ka[3], *wq = (const char*)ka[4], *w1 = (const char*)ka[5], *wo = (const char*)ka[6], *w2 = (const char*)ka[7];
  char *ws = (char*)ka[2], *out = (char*)ka8;

  const rsrc_t rk = mkrsrc(K + (size_t)g * HEADB, HEADB), rv = mkrsrc(V + (size_t)g * HEADB, HEADB);
  auto toff = [&](int n) -> unsigned { return wbase + (unsigned)(n >> 2) * 4194304u + (unsigned)(n & 3) * 32768u; };
  auto ldk = [&](unsigned so, int t) -> u32x4 { return __builtin_amdgcn_raw_buffer_load_b128(rk, koff + (t & 3) * 1024, so + (t >> 2) * 4096, AUX_NT); };
  auto ldv = [&](unsigned so, int t) -> u32x4 { return __builtin_amdgcn_raw_buffer_load_b128(rv, voff + (t & 3) * 1024, so + (t >> 2) * 4096, AUX_NT); };
  u32x4 ring[4][8];
  const int hw = wave - 4;
  const rsrc_t rx = mkrsrc(x, DM * 2);
  const rsrc_t rw = mkrsrc(ws, WS_END);
  unsigned tag = 0;
  auto proj = [&](const char* W, unsigned ldb, int rb, unsigned colb, unsigned dst, unsigned epoch, bool first) {
    const int r8 = lane >> 3, c8 = lane & 7;
    const u32x4 xr = __builtin_amdgcn_raw_buffer_load_b128(rx, lane * 16, 0, 0);
    const u32x4 xa = __builtin_amdgcn_raw_buffer_load_b128(rx, r8 * 32, rb * 256, 0), xb = __builtin_amdgcn_raw_buffer_load_b128(rx, r8 * 32 + 16, rb * 256, 0);
    const rsrc_t rm = mkrsrc(W, DM * ldb);
    u32x4 wr[16];
#pragma unroll
    for (int i = 0; i < 16; ++i) {
      wr[i] = __builtin_amdgcn_raw_buffer_load_b128(rm, r8 * 16 * ldb + c8 * 16, (128 * rb + i) * ldb + colb, AUX_NT);
      if (!first && (i & 1)) { SB(); asm volatile("s_waitcnt vmcnt(2)" ::: "memory"); SB(); }
    }
    SB();
    if (first) tag = __builtin_amdgcn_readfirstlane(epoch) + 1u;
    float xf[8];
#pragma unroll
    for (int k = 0; k < 4; ++k) { xf[2 * k] = bflo(xr[k]); xf[2 * k + 1] = bfhi(xr[k]); }
    const float mean = wave_sum(((xf[0] + xf[1]) + (xf[2] + xf[3])) + ((xf[4] + xf[5]) + (xf[6] + xf[7]))) * (1.f / 512.f);
    float vs = 0.f;
#pragma unroll
    for (int k = 0; k < 8; ++k) { const float d = xf[k] - mean; vs += d * d; }
    const float var = wave_sum(vs) * (1.f / 512.f);
    const float mb = rbf(mean), rden = __builtin_amdgcn_rcpf(rbf(__builtin_amdgcn_sqrtf(rbf(rbf(var) + 1e-5f))));
    auto ln2 = [&](unsigned xp) -> unsigned { return cvtpk(rbf(bflo(xp) - mb) * rden, rbf(bfhi(xp) - mb) * rden); };
    unsigned xn[8];
#pragma unroll
    for (int k = 0; k < 4; ++k) { xn[k] = ln2(xa[k]); xn[4 + k] = ln2(xb[k]); }
    if (first) STAMP(1);
    float a[8] = {0, 0, 0, 0, 0, 0, 0, 0};
#pragma unroll
    for (int k = 0; k < 8; ++k)
#pragma unroll
      for (int c = 0; c < 8; ++c) a[c] = dot2(colpair(wr[2 * k], wr[2 * k + 1], c), xn[k], a[c]);
    float c2[2];
    fold8(a, c2);
    c2[0] += dpp<ROR8>(c2[0]); c2[1] += dpp<ROR8>(c2[1]);
    if (li < 8) __builtin_amdgcn_raw_buffer_store_b128(u32x4{__float_as_uint(c2[0]), tag, __float_as_uint(c2[1]), tag}, rw, lg * 128 + c8 * 16, dst, AUX_SC1);
  };
  if (wave < 4) {
    if (SDELAY) __builtin_amdgcn_s_sleep(SDELAY);
#pragma unroll
    for (int i = 0; i < QPOS; ++i) {
      if (i >= WIN0) PACE();
      ring[i >> 3][i & 7] = ldk(toff(i >> 3), i & 7);
      SB();
      if (i == 7) STAMP(1);
    }
    STAMP(2);
  }
  int wave1 = wave;
  asm volatile("" : "+s"(wave1));
  if (wave1 >= 4) {
    const unsigned epoch = __builtin_amdgcn_raw_buffer_load_b32(rw, 0, WS_EPOCH, AUX_SC1);
    if (wave1 == 4) {
      proj(wq, 8192u, j >> 3, g * 1024 + (j & 7) * 128, WS_Q + g * QSTR + (j >> 3) * 4096 + (j & 7) * 512, epoch, true);
      if (lane == 0) *(volatile LAS unsigned*)sm_tag = tag;
      STAMP(2);
    } else {
      tag = __builtin_amdgcn_readfirstlane(epoch) + 1u;
#pragma unroll 1
      for (int spin = 0; spin < 4096 && __builtin_amdgcn_readfirstlane(*(volatile LAS unsigned*)sm_tag) != tag; ++spin) __builtin_amdgcn_s_sleep(1);
    }
    __builtin_amdgcn_s_sleep(QSLEEP);
    u32x4 pq[4];
    gather4<0, 0, false>(pq, ws + WS_Q + g * QSTR + hw * 4096, lane * 16, tag, ~tag, 0);
#pragma unroll
    for (int i = 0; i < 4; ++i) *reinterpret_cast<u32x2*>(sm_q + hw * 512 + i * 128 + (lane >> 5) * 64 + (lane & 7) * 8 + ((lane >> 3) & 3) * 2) = u32x2{pq[i][0], pq[i][2]};
    VMDONE();
    STAMP(11);
  }
  BARRIER();
  STAMP(3);
  int wave2 = wave;
  asm volatile("" : "+s"(wave2));
  if (wave2 < 4) {
    u32x4 qb;
    {
      const float* qp = sm_q + (li & 3) * 128 + 8 * (4 * lg + (li >> 2));
      f32x4 a = *reinterpret_cast<const f32x4*>(qp), c = *reinterpret_cast<const f32x4*>(qp + 4);
#pragma unroll
      for (int r = 1; r < 4; ++r) { a += *reinterpret_cast<const f32x4*>(qp + 512 * r); c += *reinterpret_cast<const f32x4*>(qp + 512 * r + 4); }
      qb = u32x4{cvtpk(a[0], a[1]), cvtpk(a[2], a[3]), cvtpk(c[0], c[1]), cvtpk(c[2], c[3])};
    }
    const unsigned tag16 = __builtin_amdgcn_readfirstlane(*sm_tag) & 0xffffu;
#pragma unroll
    for (int i = QPOS; i < 32; ++i) { PACE(); ring[i >> 3][i & 7] = ldk(toff(i >> 3), i & 7); SB(); }
    u64 tbl, tgtA, tgtB;
    asm volatile("s_getpc_b64 %0\n\ts_branch 1f\n\t.set w, 0\n\t.rept 16\n\ts_waitcnt vmcnt(w)\n\ts_setpc_b64 s[96:97]\n\t.set w, w+1\n\t.endr\n1:" : "=s"(tbl));
    auto steer = [&](int n) {
      const int lag = (int)((unsigned)__builtin_amdgcn_s_memrealtime() - tstart) - (CT0 + n * CP);
      const int w2 = min(max(CW0 + ((lag * CG) >> 8), CMIN), CMAX);
      tgtA = tbl + (unsigned)(8 * (w2 >> 1) - 4); tgtB = tbl + (unsigned)(8 * ((w2 + 1) >> 1) - 4);
    };
    u32x4* const sp = reinterpret_cast<u32x4*>(sm_s) + wave * (NT * 64) + lane;
    float mraw = -INFINITY;
#pragma unroll 1
    for (int it = 0; it < NT; it += 4) {
      const bool last = it == NT - 4;
      const rsrc_t rn = mkrsrc(last ? V + (size_t)g * HEADB : K + (size_t)g * HEADB, HEADB);
      const unsigned noff = last ? voff : koff;
#pragma unroll
      for (int u = 0; u < 4; ++u) {
        if (!(u & 1)) steer((it + u) >> 1);
        const unsigned so = toff((it + u + 4) & (NT - 1));
        u32x4 sc;
        float v[2];
#pragma unroll
        for (int t = 0; t < 8; ++t) {
          const f32x4 d = mfma(ring[u][t], qb, f32x4{0, 0, 0, 0});
          PACEV(t);
          ring[u][t] = __builtin_amdgcn_raw_buffer_load_b128(rn, noff + (t & 3) * 1024, so + (t >> 2) * 4096, AUX_NT);
          SB();
          v[t & 1] = ((d[0] + dpp<SHL4>(d[1])) + dpp<SHL8>(d[2])) + dpp<SHL12>(d[3]);
          if (t & 1) {
            const unsigned r = cvtpk(v[0], v[1]);
            sc[t >> 1] = cvtpk(bflo(r) * SCALE, bfhi(r) * SCALE);
            mraw = fmaxf(mraw, fmaxf(v[0], v[1]));
          }
        }
        sp[(it + u) * 64] = sc;
        if (u & 1) PROG((it + u) >> 1);
      }
    }
    const float m = rbf(rbf(xrow_max(mraw)) * SCALE), ml2 = m * L2E;
    f32x4 acc[8], accl = {0, 0, 0, 0};
#pragma unroll
    for (int e = 0; e < 8; ++e) acc[e] = f32x4{0, 0, 0, 0};
    const u32x4 ones = {0x3f803f80u, 0x3f803f80u, 0x3f803f80u, 0x3f803f80u};
    auto pv = [&](const u32x4 (&vc)[8], u32x4 (&vn)[8], int n, auto refill) {
      constexpr bool R = decltype(refill)::value;
      const unsigned so = toff(n + 3);
#define VSTEP(t) do { if constexpr (R) { PACEV(t); vn[t] = ldv(so, t); SB(); } } while (0)
      const u32x4 sc = sp[n * 64];
      VSTEP(0);
      u32x4 pb;
#pragma unroll
      for (int k = 0; k < 4; ++k) {
        pb[k] = cvtpk(__builtin_amdgcn_exp2f(__builtin_fmaf(bflo(sc[k]), L2E, -ml2)), __builtin_amdgcn_exp2f(__builtin_fmaf(bfhi(sc[k]), L2E, -ml2)));
        if (k & 1) VSTEP(1 + (k >> 1));
      }
      accl = mfma(ones, pb, accl);
      VSTEP(3);
      if constexpr (!R) SB();
#pragma unroll
      for (int e = 0; e < 8; ++e) {
        u32x4 f;
#pragma unroll
        for (int k = 0; k < 4; ++k) f[k] = colpair(vc[2 * k], vc[2 * k + 1], e);
        acc[e] = mfma(f, pb, acc[e]);
        if (e & 1) VSTEP(4 + (e >> 1));
      }
#undef VSTEP
    };
    using T = std::integral_constant<bool, true>;
    using F = std::integral_constant<bool, false>;
    pv(ring[0], ring[3], 0, F{});
#pragma unroll 1
    for (int it = 1; it < NT - 3; it += 4) {
      steer(8 + (it >> 1));
      pv(ring[1], ring[0], it, T{});
      pv(ring[2], ring[1], it + 1, T{});
      PROG(8 + (it >> 1));
      steer(9 + (it >> 1));
      pv(ring[3], ring[2], it + 2, T{});
      pv(ring[0], ring[3], it + 3, T{});
      PROG(9 + (it >> 1));
    }
    STAMP(4);
    BARRIER();
    pv(ring[1], ring[0], NT - 3, F{});
    pv(ring[2], ring[0], NT - 2, F{});
    pv(ring[3], ring[0], NT - 1, F{});
    STAMP(5);
    if (li < 4) {
      float* a = sm_acc + (wave * 4 + li) * 132 + 32 * lg;
#pragma unroll
      for (int r = 0; r < 4; ++r) {
        *reinterpret_cast<f32x4*>(a + 8 * r) = f32x4{acc[0][r], acc[1][r], acc[2][r], acc[3][r]};
        *reinterpret_cast<f32x4*>(a + 8 * r + 4) = f32x4{acc[4][r], acc[5][r], acc[6][r], acc[7][r]};
      }
      if (lg == 0) *reinterpret_cast<f32x2*>(sm_ml + (wave * 4 + li) * 2) = f32x2{m, accl[0]};
    }
    BARRIER();
    STAMP(6);
    {
      f32x2 ml[4], av[4];
#pragma unroll
      for (int w = 0; w < 4; ++w) { ml[w] = *reinterpret_cast<const f32x2*>(sm_ml + (w * 4 + wave) * 2); av[w] = *reinterpret_cast<const f32x2*>(sm_acc + (w * 4 + wave) * 132 + 2 * lane); }
      const float M = fmaxf(fmaxf(ml[0][0], ml[1][0]), fmaxf(ml[2][0], ml[3][0]));
      float L = 0.f, o0 = 0.f, o1 = 0.f;
#pragma unroll
      for (int w = 0; w < 4; ++w) { const float wt = __builtin_amdgcn_exp2f((ml[w][0] - M) * L2E); L += wt * ml[w][1]; o0 += wt * av[w][0]; o1 += wt * av[w][1]; }
      const float rl = __builtin_amdgcn_rcpf(L);
      const unsigned lse = __float_as_uint(M * L2E + __builtin_amdgcn_logf(L));
      __builtin_amdgcn_raw_buffer_store_b128(u32x4{__float_as_uint(o0 * rl), (lse & 0xffff0000u) | tag16, __float_as_uint(o1 * rl), (lse << 16) | tag16}, mkrsrc(ws, WS_END),
                                             (lane >> 3) * 4096 + (lane & 7) * 16, WS_E1 + (g * 32 + wave * 8) * 4096 + j * 128, AUX_SC1);
    }
    STAMP(7);
    BARRIER();
    STAMP_OUT();
    return;
  }

  if (hw == 1 && j >= 18) {
    const int item = g * 14 + j - 18;
    proj(w1, DFF * 2, item & 3, (item >> 2) * 128, WS_H + (item & 3) * 14336 + (item >> 2) * 512, 0u, false);
  }
  if (j >= 20) { STAMP_OUT(); VMDONE(); return; }
  const bool cons = j < 16;
  float yf[8] = {0, 0, 0, 0, 0, 0, 0, 0};
  if (j < 14) {
    const int ub = g * 14 + j;
    const rsrc_t r2 = mkrsrc(w2, DFF * DM * 2);
    for (int q = 0; q < 4; ++q) __builtin_amdgcn_s_sleep(127);
    u32x4 w2r[4];
#pragma unroll
    for (int i = 0; i < 4; ++i) w2r[i] = __builtin_amdgcn_raw_buffer_load_b128(r2, lg * 4096 + li * 16, (ub * 16 + i) * 1024 + hw * 256, AUX_NT);
    const int u = ub * 16 + 4 * lg + 2 * (li & 1);
    u32x4 ph;
#pragma unroll 1
    for (int spin = 0; spin < SPIN_CAP; ++spin) {
      ph = poll1(ws, WS_H + (li >> 2) * 14336 + (u >> 6) * 512 + ((u >> 1) & 3) * 128 + ((u >> 3) & 7) * 16);
      if (__builtin_amdgcn_ballot_w64(((ph[1] ^ tag) | (ph[3] ^ tag)) != 0) == 0) break;
      __builtin_amdgcn_s_sleep(32);
    }
    float h0 = __uint_as_float(ph[0]), h1 = __uint_as_float(ph[2]);
    h0 += dpp<ROR8>(h0); h1 += dpp<ROR8>(h1); h0 += dpp<ROR4>(h0); h1 += dpp<ROR4>(h1);
    const float hp = __uint_as_float(cvtpk(fmaxf(h0, 0.f), fmaxf(h1, 0.f)));
    const unsigned hk[2] = {__float_as_uint(dpp<0x00>(hp)), __float_as_uint(dpp<0x55>(hp))};
#pragma unroll
    for (int k = 0; k < 2; ++k)
#pragma unroll
      for (int c = 0; c < 8; ++c) yf[c] = dot2(colpair(w2r[2 * k], w2r[2 * k + 1], c), hk[k], yf[c]);
  }
  const int f = (j - 16) * 8 + g;
  const int fcol = 128 * (f >> 3) + 64 * (f & 1) + 8 * (lane & 7) + 2 * ((f >> 1) & 3);
  unsigned xo = 0;
  if (!cons) xo = __builtin_amdgcn_raw_buffer_load_b32(rx, fcol * 2, 0, 0);
  const char* const gbase = ws + (cons ? WS_E1 + (g * 32 + 2 * j + (hw & 1)) * 4096 : WS_E2 + f * 16384 + hw * 4096);
  const bool poller = !cons || hw < 2;
  u32x4 wor[8];
  auto load_wo = [&](bool on) {
    const rsrc_t ro = mkrsrc(wo, on ? 4096 * DM * 2 : 0);
#pragma unroll
    for (int i = 0; i < 8; ++i) wor[i] = __builtin_amdgcn_raw_buffer_load_b128(ro, lg * 8192 + li * 16, (g * 512 + j * 32 + i) * 1024 + hw * 256, AUX_NT);
  };
  if (poller && (PREWARM & (cons ? 1 : 2))) {
#pragma unroll
    for (int i = 0; i < 4; ++i) { u32x4 d = poll1(gbase, lane * 16 + i * 1024); asm volatile("" :: "v"(d)); }
  }
#pragma unroll 1
  for (int pass = DRY ? 0 : 1; pass < 2; ++pass) {
    if (pass) { STAMP(4); BARRIER(); STAMP(5); }
    load_wo(cons && pass);
    u32x4 pa[4];
    gather4<THR1, SLP1, true>(pa, gbase, lane * 16, tag, __builtin_amdgcn_readfirstlane((pass && poller) ? ~tag : tag), pass);
    STAMP(7);
    if (cons) {
      if (hw < 2) {
        float ls[4];
#pragma unroll
        for (int i = 0; i < 4; ++i) ls[i] = __uint_as_float(perm_hi(pa[i][1], pa[i][3]));
        float M = fmaxf(fmaxf(ls[0], ls[1]), fmaxf(ls[2], ls[3]));
        M = xrow_max(fmaxf(M, dpp<ROR8>(M)));
        float L = 0.f, o0 = 0.f, o1 = 0.f;
#pragma unroll
        for (int i = 0; i < 4; ++i) { const float wt = __builtin_amdgcn_exp2f(ls[i] - M); L += wt; o0 += wt * __uint_as_float(pa[i][0]); o1 += wt * __uint_as_float(pa[i][2]); }
        L = xrow_sum(L + dpp<ROR8>(L)); o0 = xrow_sum(o0 + dpp<ROR8>(o0)); o1 = xrow_sum(o1 + dpp<ROR8>(o1));
        const float rl = __builtin_amdgcn_rcpf(L);
        if (lane < 8) sm_o[hw * 8 + lane] = __uint_as_float(cvtpk(o0 * rl, o1 * rl));
      }
      unsigned wp[4][8];
#pragma unroll
      for (int k = 0; k < 4; ++k)
#pragma unroll
        for (int c = 0; c < 8; ++c) { wp[k][c] = colpair(wor[2 * k], wor[2 * k + 1], c); asm volatile("" : "+v"(wp[k][c])); }
      if (pass) BARRIER();
      const u32x4 op = *reinterpret_cast<const u32x4*>(sm_o + 4 * lg);
      float y[8];
#pragma unroll
      for (int c = 0; c < 8; ++c) y[c] = yf[c];
#pragma unroll
      for (int k = 0; k < 4; ++k)
#pragma unroll
        for (int c = 0; c < 8; ++c) y[c] = dot2(wp[k][c], op[k], y[c]);
      float c2[2];
      fold8(y, c2);
      __builtin_amdgcn_raw_buffer_store_b128(u32x4{__float_as_uint(c2[0]), tag, __float_as_uint(c2[1]), tag}, mkrsrc(ws, pass ? WS_END : 0),
                                             (lane >> 3) * 16384 + (lane & 7) * 16, WS_E2 + hw * 8 * 16384 + (g * 16 + j) * 128, AUX_SC1);
      STAMP(8);
    } else {
      float s0 = (__uint_as_float(pa[0][0]) + __uint_as_float(pa[1][0])) + (__uint_as_float(pa[2][0]) + __uint_as_float(pa[3][0]));
      float s1 = (__uint_as_float(pa[0][2]) + __uint_as_float(pa[1][2])) + (__uint_as_float(pa[2][2]) + __uint_as_float(pa[3][2]));
      s0 = xrow_sum(s0 + dpp<ROR8>(s0)); s1 = xrow_sum(s1 + dpp<ROR8>(s1));
      if (lane < 8) *reinterpret_cast<f32x2*>(sm_o + hw * 16 + 2 * lane) = f32x2{s0, s1};
      if (pass) BARRIER();
      STAMP(9);
      f32x2 r[4];
#pragma unroll
      for (int w = 0; w < 4; ++w) r[w] = *reinterpret_cast<const f32x2*>(sm_o + w * 16 + 2 * (lane & 7));
      const f32x2 t = (r[0] + r[1]) + (r[2] + r[3]);
      __builtin_amdgcn_raw_buffer_store_b32(cvtpk(bflo(xo) + t[0], bfhi(xo) + t[1]), mkrsrc(out, (pass && hw == 0) ? DM * 2 : 0), lane < 8 ? fcol * 2 : DM * 2, 0, 0);
      __builtin_amdgcn_raw_buffer_store_b32(tag, mkrsrc(ws, (pass && hw == 0 && f == 0) ? 4 : 0), lane * 4, WS_EPOCH, AUX_SC1);
      STAMP(10);
    }
  }
  STAMP_OUT();
  VMDONE();
}

extern "C" void kernel_launch(void* const* d_in, const int* in_sizes, int n_in, void* d_out, int out_size, void* d_ws, size_t ws_size,
                              hipStream_t stream) {
  fused_decode<<<256, 512, 0, stream>>>(d_in[1], d_in[2], d_ws, d_in[0], d_in[3], d_in[6], d_in[4], d_in[7], d_out);
}
```

```cpp
#include <hip/hip_runtime.h>
#include <hip/hip_bf16.h>
#include <stdint.h>

#define KNOB(name, val) constexpr int name = val;
#define WIN 6
#define QPOS 26
#define QSLEEP 30
#define LSLEEP 110
#define E1DELAY 12
#define E2DELAY 40
#define CP 217
#define CT0 460
#define CG 8
#define CW0 11
#define CMIN 8
#define CMAX 16
#ifndef NVOTE
#define NVOTE 4
#endif
#define FORCE -1
#define CODEPF_BYTES 20224
#define STAMPS 0

typedef unsigned short u16;
typedef unsigned long long u64;
typedef unsigned u32x4 __attribute__((ext_vector_type(4)));
typedef unsigned u32x2 __attribute__((ext_vector_type(2)));
typedef float f32x4 __attribute__((ext_vector_type(4)));
typedef float f32x2 __attribute__((ext_vector_type(2)));
typedef __bf16 bf16x8 __attribute__((ext_vector_type(8)));
typedef __bf16 bf16x2 __attribute__((ext_vector_type(2)));
typedef u64 u64x8 __attribute__((ext_vector_type(8)));
typedef __amdgpu_buffer_rsrc_t rsrc_t;
#define LAS __attribute__((address_space(3)))

constexpr int DM = 512, SK = 65536, DH = 128, DFF = 1792;
constexpr int NT = 16;
constexpr unsigned HEADB = SK * DH * 2;
constexpr unsigned WS_EPOCH = 0;
constexpr unsigned WS_V = 4096;
constexpr unsigned WS_Q = 16384;
constexpr unsigned WS_H = WS_Q + 8 * 16384;
constexpr unsigned WS_S = WS_H + 65536;
constexpr unsigned WS_P = WS_S + 8 * 4096;
constexpr unsigned WS_E1 = WS_P + 8 * 16384;
constexpr unsigned WS_E2 = WS_E1 + 64 * 16384;
constexpr unsigned WS_END = WS_E2 + 32 * 16384;
static_assert(WS_P % 16384 == 0 && WS_END <= (2u << 20), "layout");
__device__ __forceinline__ unsigned blk(unsigned base, unsigned i, unsigned half) { return base + (i >> 1) * 16384 + (i & 1) * 4096 + half * 8192; }
constexpr unsigned WS_STAMP = 16u << 20;
constexpr int ACCL = 36, ACCH = 4 * ACCL + 4;
constexpr int SPIN_CAP = 1 << 18;
constexpr int AUX_NT = 2, AUX_SC1 = 16;
constexpr float L2E = 1.4426950408889634f, SCALE = 0.088388347648318440f;

#define LAY "jjnjjjnwwnn"
template <char C> __device__ __forceinline__ unsigned deposit(unsigned v) {
  unsigned r = 0; int k = 0;
#pragma unroll
  for (int i = 0; i < 11; ++i) if (LAY[i] == C) { r |= ((v >> k) & 1u) << (13 + i); ++k; }
  return r;
}
__device__ __forceinline__ float bflo(unsigned v) { return __uint_as_float(v << 16); }
__device__ __forceinline__ float bfhi(unsigned v) { return __uint_as_float(v & 0xffff0000u); }
__device__ __forceinline__ unsigned cvtpk(float lo, float hi) {
  bf16x2 r; r[0] = (__bf16)lo; r[1] = (__bf16)hi; return __builtin_bit_cast(unsigned, r);
}
__device__ __forceinline__ float rbf(float f) { return bflo(cvtpk(f, f)); }
template <int DC> __device__ __forceinline__ float dpp(float x) {
  return __builtin_bit_cast(float, __builtin_amdgcn_mov_dpp(__builtin_bit_cast(int, x), DC, 0xf, 0xf, true));
}
constexpr int ROR1 = 0x121, ROR2 = 0x122, ROR4 = 0x124, ROR8 = 0x128, SHL4 = 0x104, SHL8 = 0x108, SHL12 = 0x10c;
__device__ __forceinline__ float xrow_max(float x) {
  auto s = __builtin_amdgcn_permlane16_swap(__float_as_uint(x), __float_as_uint(x), false, false);
  x = fmaxf(__uint_as_float(s[0]), __uint_as_float(s[1]));
  auto t = __builtin_amdgcn_permlane32_swap(__float_as_uint(x), __float_as_uint(x), false, false);
  return fmaxf(__uint_as_float(t[0]), __uint_as_float(t[1]));
}
__device__ __forceinline__ float xrow_sum(float x) {
  auto s = __builtin_amdgcn_permlane16_swap(__float_as_uint(x), __float_as_uint(x), false, false);
  x = __uint_as_float(s[0]) + __uint_as_float(s[1]);
  auto t = __builtin_amdgcn_permlane32_swap(__float_as_uint(x), __float_as_uint(x), false, false);
  return __uint_as_float(t[0]) + __uint_as_float(t[1]);
}
__device__ __forceinline__ float row_sum(float x) {
  x += dpp<ROR8>(x); x += dpp<ROR4>(x); x += dpp<ROR2>(x); x += dpp<ROR1>(x); return x;
}
__device__ __forceinline__ float wave_sum(float x) { return xrow_sum(row_sum(x)); }
__device__ __forceinline__ float dot2(unsigned a, unsigned b, float c) {
  return __builtin_amdgcn_fdot2_f32_bf16(__builtin_bit_cast(bf16x2, a), __builtin_bit_cast(bf16x2, b), c, false);
}
__device__ __forceinline__ unsigned perm_lo(unsigned hi, unsigned lo) { return __builtin_amdgcn_perm(hi, lo, 0x05040100u); }
__device__ __forceinline__ unsigned perm_hi(unsigned hi, unsigned lo) { return __builtin_amdgcn_perm(hi, lo, 0x07060302u); }
__device__ __forceinline__ unsigned colpair(const u32x4& r0, const u32x4& r1, int c) {
  return (c & 1) ? perm_hi(r1[c >> 1], r0[c >> 1]) : perm_lo(r1[c >> 1], r0[c >> 1]);
}
__device__ __forceinline__ rsrc_t mkrsrc(const void* p, unsigned bytes) { return __builtin_amdgcn_make_buffer_rsrc(const_cast<void*>(p), 0, bytes, 0x00020000); }
__device__ __forceinline__ f32x4 mfma(u32x4 a, u32x4 b, f32x4 c) {
  return __builtin_amdgcn_mfma_f32_16x16x32_bf16(__builtin_bit_cast(bf16x8, a), __builtin_bit_cast(bf16x8, b), c, 0, 0, 0);
}
__device__ __forceinline__ void fold8(const float (&a)[8], float (&c2)[2]) {
  float b4[4];
#pragma unroll
  for (int k = 0; k < 4; ++k) { auto r = __builtin_amdgcn_permlane32_swap(__float_as_uint(a[k]), __float_as_uint(a[k + 4]), false, false); b4[k] = __uint_as_float(r[0]) + __uint_as_float(r[1]); }
#pragma unroll
  for (int k = 0; k < 2; ++k) { auto r = __builtin_amdgcn_permlane16_swap(__float_as_uint(b4[k]), __float_as_uint(b4[k + 2]), false, false); c2[k] = __uint_as_float(r[0]) + __uint_as_float(r[1]); }
}

__device__ __forceinline__ u32x2 pack2(float a, float b, unsigned tag) {
  return u32x2{__builtin_amdgcn_perm(__float_as_uint(a), __float_as_uint(b), 0x07060503u), __builtin_amdgcn_perm(__float_as_uint(b), tag, 0x06050100u)};
}
__device__ __forceinline__ float unpackA(unsigned w0) { return __uint_as_float(w0 & 0xffffff00u); }
__device__ __forceinline__ float unpackB(unsigned w0, unsigned w1) { return __uint_as_float(__builtin_amdgcn_perm(w0, w1, 0x0403020cu)); }
__device__ __forceinline__ f32x2 sum_pieces(const u32x4 (&p)[4]) {
  float v[4];
  v[0] = (unpackA(p[0][0]) + unpackA(p[1][0])) + (unpackA(p[2][0]) + unpackA(p[3][0]));
  v[1] = (unpackB(p[0][0], p[0][1]) + unpackB(p[1][0], p[1][1])) + (unpackB(p[2][0], p[2][1]) + unpackB(p[3][0], p[3][1]));
  v[2] = (unpackA(p[0][2]) + unpackA(p[1][2])) + (unpackA(p[2][2]) + unpackA(p[3][2]));
  v[3] = (unpackB(p[0][2], p[0][3]) + unpackB(p[1][2], p[1][3])) + (unpackB(p[2][2], p[2][3]) + unpackB(p[3][2], p[3][3]));
  f32x2 r;
#pragma unroll
  for (int k = 0; k < 2; ++k) {
    auto t = __builtin_amdgcn_permlane32_swap(__float_as_uint(v[k]), __float_as_uint(v[k + 2]), false, false);
    float x = __uint_as_float(t[0]) + __uint_as_float(t[1]);
    x += dpp<ROR8>(x);
    auto u = __builtin_amdgcn_permlane16_swap(__float_as_uint(x), __float_as_uint(x), false, false);
    r[k] = __uint_as_float(u[0]) + __uint_as_float(u[1]);
  }
  return r;
}

template <int SLP>
__device__ __forceinline__ void gather4(u32x4 (&a)[4], const char* base, unsigned off, unsigned tag) {
  u64 m0, m1, m2, m3, t0; int cnt = SPIN_CAP;
  asm volatile(
      "s_mov_b64 %[m0], -1\n\ts_mov_b64 %[m1], -1\n\ts_mov_b64 %[m2], -1\n\ts_mov_b64 %[m3], -1\n\ts_nop 0\n"
      "0:\n\t"
      "s_wqm_b64 exec, %[m0]\n\tglobal_load_dwordx4 v[40:43], %[off], %[base] sc1\n\t"
      "s_wqm_b64 exec, %[m1]\n\tglobal_load_dwordx4 v[44:47], %[off], %[base] offset:1024 sc1\n\t"
      "s_wqm_b64 exec, %[m2]\n\tglobal_load_dwordx4 v[48:51], %[off], %[base] offset:2048 sc1\n\t"
      "s_wqm_b64 exec, %[m3]\n\tglobal_load_dwordx4 v[52:55], %[off], %[base] offset:3072 sc1\n\t"
      "s_mov_b64 exec, -1\n\t"
      "s_sub_u32 %[cnt], %[cnt], 1\n\ts_cbranch_scc1 9f\n\t"
      "s_waitcnt vmcnt(0)\n\t"
      "v_bitop3_b32 v56, v41, v43, %[tag] bitop3:0x7e\n\tv_bitop3_b32 v57, v45, v47, %[tag] bitop3:0x7e\n\t"
      "v_bitop3_b32 v58, v49, v51, %[tag] bitop3:0x7e\n\tv_bitop3_b32 v59, v53, v55, %[tag] bitop3:0x7e\n\t"
      "v_cmp_ne_u16 %[m0], 0, v56\n\tv_cmp_ne_u16 %[m1], 0, v57\n\tv_cmp_ne_u16 %[m2], 0, v58\n\tv_cmp_ne_u16 %[m3], 0, v59\n\t"
      ".if %c[slp]\n\ts_sleep %c[slp]\n\t.endif\n\t"
      "s_or_b64 %[t0], %[m0], %[m1]\n\ts_or_b64 vcc, %[m2], %[m3]\n\ts_or_b64 %[t0], %[t0], vcc\n\t"
      "s_cbranch_scc1 0b\n"
      "9:\n\ts_waitcnt vmcnt(0)"
      : "=&{v[40:43]}"(a[0]), "=&{v[44:47]}"(a[1]), "=&{v[48:51]}"(a[2]), "=&{v[52:55]}"(a[3]),
        [m0] "=&s"(m0), [m1] "=&s"(m1), [m2] "=&s"(m2), [m3] "=&s"(m3), [t0] "=&s"(t0), [cnt] "+s"(cnt)
      : [tag] "s"(tag), [base] "s"(base), [off] "v"(off), [slp] "i"(SLP)
      : "memory", "vcc", "scc", "v56", "v57", "v58", "v59");
}
__device__ __forceinline__ u32x4 load_dev(const char* base, unsigned off) {
  u32x4 a;
  asm volatile("global_load_dwordx4 %0, %1, %2 sc1\n\ts_waitcnt vmcnt(0)" : "=&v"(a) : "v"(off), "s"(base) : "memory");
  return a;
}
__device__ __forceinline__ u32x4 gather1(const char* base, unsigned off, unsigned tag) {
  u32x4 p;
#pragma unroll 1
  for (int spin = 0; spin < SPIN_CAP; ++spin) {
    p = load_dev(base, off);
    if (__builtin_amdgcn_ballot_w64(((p[1] ^ tag) | (p[3] ^ tag)) != 0) == 0) break;
    __builtin_amdgcn_s_sleep(32);
  }
  return p;
}
template <int SLP>
__device__ __forceinline__ void lds_wait(const unsigned* flags, int idx) {
#pragma unroll 1
  for (int spin = 0; spin < SPIN_CAP; ++spin) {
    if (__builtin_amdgcn_ballot_w64(*(volatile LAS unsigned*)(flags + idx) == 0u) == 0) break;
    __builtin_amdgcn_s_sleep(SLP);
  }
  asm volatile("" ::: "memory");
}
__device__ __forceinline__ void lds_set(unsigned* flag) {
  asm volatile("" ::: "memory");
  *(volatile LAS unsigned*)flag = 1u;
}
__device__ __forceinline__ int timed_read(const char* p) {
  u64 t0, t1; unsigned d;
  asm volatile("s_memtime %0\n\ts_waitcnt lgkmcnt(0)\n\ts_load_dword %2, %3, 0\n\ts_waitcnt lgkmcnt(0)\n\ts_memtime %1\n\ts_waitcnt lgkmcnt(0)"
               : "=&s"(t0), "=&s"(t1), "=&s"(d) : "s"(p) : "memory");
  return (int)((unsigned)t1 - (unsigned)t0);
}

#define SB() __builtin_amdgcn_sched_barrier(0)
#define PIN(x) asm volatile("" : "+v"(x))
#define PACE() do { SB(); asm volatile("s_waitcnt vmcnt(%0)" :: "i"(WIN - 1) : "memory"); } while (0)
#define PACEV(t) do { SB(); asm volatile("s_swappc_b64 s[96:97], %0" :: "s"(((t) & 1) ? tgtB : tgtA) : "memory", "s96", "s97"); } while (0)
#define VMDONE() __builtin_amdgcn_s_waitcnt(0x0f70)
#define BARRIER() do { SB(); asm volatile("s_waitcnt lgkmcnt(0)\n\ts_barrier" ::: "memory"); SB(); } while (0)
#define STAMP(i)
#define PROG(i)
#define STAMP_OUT()

__global__ void __launch_bounds__(512, 2) __attribute__((aligned(4096)))
fused_decode(const void* K_, const void* V_, void* ws_, const void* x_, const void* wq_, const void* w1_, const void* wo_, const void* w2_, void* out_) {
  __shared__ __attribute__((aligned(16))) float lds[2048 + 16 * ACCH + 128 + 48 + 4 * NT * 256];
  float* const sm_q = lds;
  float* const sm_acc = lds + 2048;
  float* const sm_o = sm_acc + 16 * ACCH;
  float* const sm_m = sm_o + 128;
  float* const sm_lse = sm_m + 16;
  unsigned* const sm_w = reinterpret_cast<unsigned*>(sm_lse + 4);
  unsigned* const sm_flag = sm_w + 12;
  float* const sm_s = sm_m + 48;
  asm volatile("s_load_dwordx16 s[64:79], %0, 0\n\ts_load_dwordx2 s[80:81], %0, 0x40" :: "s"(__builtin_amdgcn_kernarg_segment_ptr())
               : "s64", "s65", "s66", "s67", "s68", "s69", "s70", "s71", "s72", "s73", "s74", "s75", "s76", "s77", "s78", "s79", "s80", "s81");
  SB();
  const int b = blockIdx.x, g = b & 7, j = b >> 3;
  const int tid = threadIdx.x, lane = tid & 63, wave = __builtin_amdgcn_readfirstlane(tid >> 6);
  const int lg = lane >> 4, li = lane & 15;
  unsigned koff = (li >> 2) * 256 + lg * 64 + (li & 3) * 16, voff = lane * 16, wbase = deposit<'j'>(j) | deposit<'w'>(wave);
  unsigned qoff = (lane >> 3) * 16 * 8192 + (lane & 7) * 16, xoff = (lane >> 3) * 32;
  if (tid < 16) sm_flag[tid] = 0u;
  u64x8 ka; u64 ka8;
  {
    u64 pc;
    asm volatile("s_cmp_lg_u32 %8, 7\n\ts_cbranch_scc1 1f\n\ts_getpc_b64 %2\n\ts_lshr_b64 %2, %2, 12\n\ts_lshl_b64 %2, %2, 12\n\tglobal_load_dword v255, %7, %2\n1:\n\t"
                 "s_waitcnt lgkmcnt(0)"
                 : "={s[64:79]}"(ka), "={s[80:81]}"(ka8), "=&s"(pc), "+v"(koff), "+v"(voff), "+v"(qoff), "+v"(xoff)
                 : "v"(min(j * 1024 + (lane & 7) * 128, CODEPF_BYTES - 4)), "s"(wave) : "memory", "scc", "v255");
  }
  STAMP(0);
  const char *K = (const char*)ka[0], *V = (const char*)ka[1], *x = (const char*)ka[3], *wq = (const char*)ka[4], *w1 = (const char*)ka[5], *wo = (const char*)ka[6], *w2 = (const char*)ka[7];
  char *ws = (char*)((ka[2] + 0x1fffffull) & ~0x1fffffull), *out = (char*)ka8;
  const int hw = wave - 4;
  const rsrc_t rx = mkrsrc(x, DM * 2);
  const rsrc_t rw = mkrsrc(ws, WS_END);
  unsigned tag = 0;

  u32x4 xr, xa, xb, wr[16];
  auto proj_load = [&](const char* W, unsigned ldb, unsigned woff, int rb, unsigned colb, bool paced) {
    xr = __builtin_amdgcn_raw_buffer_load_b128(rx, voff, 0, 0);
    xa = __builtin_amdgcn_raw_buffer_load_b128(rx, xoff, rb * 256, 0); xb = __builtin_amdgcn_raw_buffer_load_b128(rx, xoff + 16, rb * 256, 0);
    const rsrc_t rm = mkrsrc(W, DM * ldb);
#pragma unroll
    for (int i = 0; i < 16; ++i) {
      wr[i] = __builtin_amdgcn_raw_buffer_load_b128(rm, woff, (128 * rb + i) * ldb + colb, AUX_NT);
      if (paced && (i & 1)) { SB(); asm volatile("s_waitcnt vmcnt(2)" ::: "memory"); SB(); }
    }
  };
  auto proj_send = [&](unsigned dst) {
    float xf[8];
#pragma unroll
    for (int k = 0; k < 4; ++k) { xf[2 * k] = bflo(xr[k]); xf[2 * k + 1] = bfhi(xr[k]); }
    const float mean = wave_sum(((xf[0] + xf[1]) + (xf[2] + xf[3])) + ((xf[4] + xf[5]) + (xf[6] + xf[7]))) * (1.f / 512.f);
    float vs = 0.f;
#pragma unroll
    for (int k = 0; k < 8; ++k) { const float d = xf[k] - mean; vs += d * d; }
    const float var = wave_sum(vs) * (1.f / 512.f);
    const float mb = rbf(mean), rden = __builtin_amdgcn_rcpf(rbf(__builtin_amdgcn_sqrtf(rbf(rbf(var) + 1e-5f))));
    auto ln2 = [&](unsigned xp) -> unsigned { return cvtpk(rbf(bflo(xp) - mb) * rden, rbf(bfhi(xp) - mb) * rden); };
    unsigned xn[8];
#pragma unroll
    for (int k = 0; k < 4; ++k) { xn[k] = ln2(xa[k]); xn[4 + k] = ln2(xb[k]); }
    float a[8] = {0, 0, 0, 0, 0, 0, 0, 0};
#pragma unroll
    for (int k = 0; k < 8; ++k)
#pragma unroll
      for (int c = 0; c < 8; ++c) a[c] = dot2(colpair(wr[2 * k], wr[2 * k + 1], c), xn[k], a[c]);
    float c2[2];
    fold8(a, c2);
    c2[0] += dpp<ROR8>(c2[0]); c2[1] += dpp<ROR8>(c2[1]);
    if (li < 8) __builtin_amdgcn_raw_buffer_store_b128(u32x4{__float_as_uint(c2[0]), tag, __float_as_uint(c2[1]), tag}, rw, lg * 128 + li * 16, dst, AUX_SC1);
  };
  unsigned epoch;
  if (wave == 4) {
    epoch = __builtin_amdgcn_raw_buffer_load_b32(rw, 0, WS_EPOCH, AUX_SC1);
    proj_load(wq, 8192u, qoff, j >> 3, g * 1024 + (j & 7) * 128, false);
  }
  SB();
  asm volatile("s_barrier" ::: "memory");
  SB();

  const rsrc_t rk = mkrsrc(K + (size_t)g * HEADB, HEADB), rv = mkrsrc(V + (size_t)g * HEADB, HEADB);
  auto toff = [&](int n) -> unsigned { return wbase | deposit<'n'>(n); };
  auto ldk = [&](unsigned so, int t) -> u32x4 { return __builtin_amdgcn_raw_buffer_load_b128(rk, koff + (t & 3) * 1024, so + (t >> 2) * 4096, AUX_NT); };
  auto ldv = [&](unsigned so, int t) -> u32x4 { return __builtin_amdgcn_raw_buffer_load_b128(rv, voff + (t & 3) * 1024, so + (t >> 2) * 4096, AUX_NT); };
  u32x4 ring[4][8];
  int role = wave >> 2;
  asm volatile("" : "+s"(role));
  if (role == 0) {
#pragma unroll
    for (int i = 0; i < QPOS; ++i) {
      if (i >= WIN) PACE();
      ring[i >> 3][i & 7] = ldk(toff(i >> 3), i & 7);
      SB();
      if (i == 7) STAMP(1);
    }
    STAMP(2);
    lds_wait<1>(sm_flag, lane & 3);
    STAMP(3);
    u32x4 qb;
    {
      const float* qp = sm_q + (li & 3) * 128 + 8 * (4 * lg + (li >> 2));
      f32x4 a = *reinterpret_cast<const f32x4*>(qp), c = *reinterpret_cast<const f32x4*>(qp + 4);
#pragma unroll
      for (int r = 1; r < 4; ++r) { a += *reinterpret_cast<const f32x4*>(qp + 512 * r); c += *reinterpret_cast<const f32x4*>(qp + 512 * r + 4); }
      qb = u32x4{cvtpk(a[0], a[1]), cvtpk(a[2], a[3]), cvtpk(c[0], c[1]), cvtpk(c[2], c[3])};
    }
    tag = __builtin_amdgcn_readfirstlane(sm_w[0]);
    const unsigned tstart = __builtin_amdgcn_readfirstlane(sm_w[1]);
#pragma unroll
    for (int i = QPOS; i < 32; ++i) { PACE(); ring[i >> 3][i & 7] = ldk(toff(i >> 3), i & 7); SB(); }
    u64 tbl, tgtA, tgtB;
    asm volatile("s_getpc_b64 %0\n\ts_branch 1f\n\t.set w, 0\n\t.rept 16\n\ts_waitcnt vmcnt(w)\n\ts_setpc_b64 s[96:97]\n\t.set w, w+1\n\t.endr\n1:" : "=s"(tbl));
    auto steer = [&](int n) {
      const int lag = (int)((unsigned)__builtin_amdgcn_s_memrealtime() - tstart) - (CT0 + ((n * CP) >> 1));
      const int w2 = min(max(CW0 + ((lag * CG) >> 8), CMIN), CMAX);
      tgtA = tbl + (unsigned)(8 * (w2 >> 1) - 4); tgtB = tbl + (unsigned)(8 * ((w2 + 1) >> 1) - 4);
    };
    u32x4* const sp = reinterpret_cast<u32x4*>(sm_s) + wave * (NT * 64) + lane;
    float mraw = -INFINITY;
#pragma unroll 1
    for (int it = 0; it < NT; it += 4) {
      const bool last = it == NT - 4;
      const rsrc_t rn = mkrsrc(last ? V + (size_t)g * HEADB : K + (size_t)g * HEADB, HEADB);
      const unsigned noff = last ? voff : koff;
#pragma unroll
      for (int u = 0; u < 4; ++u) {
        if (!(u & 1)) steer(it + u);
        const unsigned so = toff((it + u + 4) & (NT - 1));
        u32x4 sc;
        float v[2];
#pragma unroll
        for (int t = 0; t < 8; ++t) {
          const f32x4 d = mfma(ring[u][t], qb, f32x4{0, 0, 0, 0});
          PACEV(t);
          ring[u][t] = __builtin_amdgcn_raw_buffer_load_b128(rn, noff + (t & 3) * 1024, so + (t >> 2) * 4096, AUX_NT);
          SB();
          v[t & 1] = ((d[0] + dpp<SHL4>(d[1])) + dpp<SHL8>(d[2])) + dpp<SHL12>(d[3]);
          if (t & 1) {
            const unsigned r = cvtpk(v[0], v[1]);
            sc[t >> 1] = cvtpk(bflo(r) * SCALE, bfhi(r) * SCALE);
            mraw = fmaxf(mraw, fmaxf(v[0], v[1]));
          }
        }
        sp[(it + u) * 64] = sc;
        if (u & 1) PROG((it + u) >> 1);
      }
    }
    const float m = rbf(rbf(xrow_max(mraw)) * SCALE);
    float ml2 = m * L2E;
    if (lane < 4) sm_m[wave * 4 + lane] = m;
    lds_set(sm_flag + 4 + wave);
    f32x4 acc[8];
#pragma unroll
    for (int e = 0; e < 8; ++e) acc[e] = f32x4{0, 0, 0, 0};
    auto probs = [&](unsigned sc) -> unsigned {
      return cvtpk(__builtin_amdgcn_exp2f(__builtin_fmaf(bflo(sc), L2E, -ml2)), __builtin_amdgcn_exp2f(__builtin_fmaf(bfhi(sc), L2E, -ml2)));
    };
    auto pv = [&](const u32x4 (&vc)[8], u32x4 (&vn)[8], int n, auto refill) {
      constexpr bool R = decltype(refill)::value;
      const unsigned so = toff(n + 3);
#define VSTEP(t) do { if constexpr (R) { PACEV(t); vn[t] = ldv(so, t); SB(); } } while (0)
      const u32x4 sc = sp[n * 64];
      VSTEP(0);
      u32x4 pb;
#pragma unroll
      for (int k = 0; k < 4; ++k) {
        pb[k] = probs(sc[k]);
        if (k & 1) VSTEP(1 + (k >> 1));
      }
      VSTEP(3);
#pragma unroll
      for (int e = 0; e < 8; ++e) {
        u32x4 f;
#pragma unroll
        for (int k = 0; k < 4; ++k) f[k] = colpair(vc[2 * k], vc[2 * k + 1], e);
        acc[e] = mfma(f, pb, acc[e]);
        if (e & 1) VSTEP(4 + (e >> 1));
      }
#undef VSTEP
    };
    using T = std::integral_constant<bool, true>;
    using F = std::integral_constant<bool, false>;
    pv(ring[0], ring[3], 0, F{});
#pragma unroll 1
    for (int it = 1; it < NT - 3; it += 4) {
      steer(15 + it);
      pv(ring[1], ring[0], it, T{});
      pv(ring[2], ring[1], it + 1, T{});
      PROG(8 + (it >> 1));
      steer(17 + it);
      pv(ring[3], ring[2], it + 2, T{});
      pv(ring[0], ring[3], it + 3, T{});
      PROG(9 + (it >> 1));
    }
    STAMP(4);
    BARRIER();
    {
      const float lse = sm_lse[li & 3], fs = __builtin_amdgcn_exp2f(ml2 - lse);
      ml2 = lse;
#pragma unroll
      for (int e = 0; e < 8; ++e) acc[e] *= fs;
    }
    const unsigned half = (sm_w[2] >> g) & 1u;
    unsigned a_wr = (unsigned)(uintptr_t)(LAS float*)(sm_acc + (wave * 4 + (li & 3)) * ACCH + lg * ACCL);
    unsigned a_l2 = (unsigned)(uintptr_t)(LAS float*)(sm_acc + (2 * wave + (lane >> 5)) * ACCH + ((lane >> 3) & 3) * ACCL + 4 * (lane & 7));
    unsigned a_l3 = (unsigned)(uintptr_t)(LAS float*)(sm_o + 2 * (lane & 7) + (lane >> 5));
    unsigned a_g = blk(WS_E1, g * 16 + wave * 8 + (lane >> 3), half) + j * 128 + (lane & 7) * 16, a_in = blk(WS_E1, g * 16 + (j & 15), half) + lane * 16;
    PIN(a_wr); PIN(a_l2); PIN(a_l3); PIN(a_g); PIN(a_in);
    pv(ring[1], ring[0], NT - 3, F{});
    pv(ring[2], ring[0], NT - 2, F{});
    {
      const u32x4 sc = sp[(NT - 1) * 64];
      u32x4 pb, f[8];
#pragma unroll
      for (int k = 0; k < 4; ++k) pb[k] = probs(sc[k]);
#pragma unroll
      for (int k = 0; k < 3; ++k) {
        SB();
#pragma unroll
        for (int e = 0; e < 8; ++e) f[e][k] = colpair(ring[3][2 * k], ring[3][2 * k + 1], e);
      }
      SB();
#pragma unroll
      for (int e = 0; e < 8; ++e) { f[e][3] = colpair(ring[3][6], ring[3][7], e); acc[e] = mfma(f[e], pb, acc[e]); }
    }
    STAMP(5);
    if (li < 4) {
#pragma unroll
      for (int e = 0; e < 8; ++e) *reinterpret_cast<LAS f32x4*>(a_wr + 16 * e) = acc[e];
    }
    BARRIER();
    STAMP(6);
    if (wave < 2) {
      const f32x4 v = (*reinterpret_cast<const LAS f32x4*>(a_l2) + *reinterpret_cast<const LAS f32x4*>(a_l2 + 16 * ACCH)) +
                      (*reinterpret_cast<const LAS f32x4*>(a_l2 + 32 * ACCH) + *reinterpret_cast<const LAS f32x4*>(a_l2 + 48 * ACCH));
      const u32x2 g0 = pack2(v[0], v[1], tag), g1 = pack2(v[2], v[3], tag);
      __builtin_amdgcn_raw_buffer_store_b128(u32x4{g0[0], g0[1], g1[0], g1[1]}, rw, a_g, 0, AUX_SC1);
      STAMP(7);
    } else if (wave == 2 && j < 16) {
      __builtin_amdgcn_s_sleep(E1DELAY);
      u32x4 pa[4];
      gather4<0>(pa, ws, a_in, tag);
      STAMP(8);
      const f32x2 o = sum_pieces(pa);
      *reinterpret_cast<LAS unsigned*>(a_l3) = cvtpk(o[0], o[1]);
    }
    BARRIER();
    STAMP_OUT();
    asm volatile("s_endpgm" : "=v"(wr[0]), "=v"(wr[1]), "=v"(wr[2]), "=v"(wr[3]), "=v"(wr[4]), "=v"(wr[5]), "=v"(wr[6]), "=v"(wr[7]), "=v"(wr[8]), "=v"(wr[9]),
                 "=v"(wr[10]), "=v"(wr[11]), "=v"(wr[12]), "=v"(wr[13]), "=v"(wr[14]), "=v"(wr[15]), "=v"(xr), "=v"(xa), "=v"(xb), "=v"(epoch) :: "memory");
  }

  if (wave == 4) {
    tag = __builtin_amdgcn_readfirstlane(epoch) + 1u;
    SB();
    const unsigned t0 = (unsigned)__builtin_amdgcn_s_memrealtime();
    *reinterpret_cast<u32x2*>(sm_w) = u32x2{tag, t0};
    STAMP(1);
    proj_send(WS_Q + g * 16384 + (j >> 3) * 4096 + (j & 7) * 512);
    lds_set(sm_flag + 8);
    STAMP(2);
  }
  lds_wait<1>(sm_flag, 8);
  tag = __builtin_amdgcn_readfirstlane(sm_w[0]);
  __builtin_amdgcn_s_sleep(QSLEEP);
  {
    u32x4 pq[4];
    gather4<0>(pq, ws, WS_Q + g * 16384 + hw * 4096 + lane * 16, tag);
#pragma unroll
    for (int i = 0; i < 4; ++i) *reinterpret_cast<u32x2*>(sm_q + hw * 512 + i * 128 + (lane >> 5) * 64 + (lane & 7) * 8 + ((lane >> 3) & 3) * 2) = u32x2{pq[i][0], pq[i][2]};
    lds_set(sm_flag + hw);
    STAMP(11);
  }
  if (hw == 1 && j >= 18) {
    const int item = g * 14 + j - 18;
    proj_load(w1, DFF * 2, (lane >> 3) * 16 * (DFF * 2) + (lane & 7) * 16, item & 3, (item >> 2) * 128, true);
    proj_send(WS_H + (item & 3) * 14336 + (item >> 2) * 512);
  }
  if (hw == 3 && j >= 24) {
    int d = 0;
#pragma unroll 1
    for (int k = 0; k < NVOTE; ++k) {
      const char* p = ws + WS_P + k * 16384 + (((j - 24 + 4 * k) & 7) * 8 + g) * 128;
      d += min(max(timed_read(p) - timed_read(p + 8192), -600), 600);
    }
    if (lane < 4) __builtin_amdgcn_raw_buffer_store_b128(u32x4{(unsigned)d, tag, (unsigned)d, tag}, rw, lane * 16, WS_V + (g * 8 + j - 24) * 128, AUX_SC1);
    __builtin_amdgcn_s_sleep(127);
  } else if (hw == 3 || j < 14) {
    for (int q = 0; q < 3; ++q) __builtin_amdgcn_s_sleep(127);
  }
  const int f = (j - 16) * 8 + g;
  if (hw == 3) {
    int d = (int)gather1(ws, WS_V + lane * 128, tag)[0];
    d += __builtin_amdgcn_mov_dpp(d, 0xB1, 0xf, 0xf, true); d += __builtin_amdgcn_mov_dpp(d, 0x4E, 0xf, 0xf, true); d += __builtin_amdgcn_mov_dpp(d, 0x141, 0xf, 0xf, true);
    unsigned halves = (unsigned)(((__builtin_amdgcn_ballot_w64(d > 0) & 0x0101010101010101ull) * 0x0102040810204080ull) >> 56);
    if (FORCE >= 0) halves = FORCE;
    sm_w[2] = halves;
    const int nb = j < 16 ? 1 : j < 18 ? 4 : 0;
#pragma unroll 1
    for (int i = 0; i < nb; ++i) {
      u32x4 dd[4];
      const unsigned o = blk(j < 16 ? WS_E1 : WS_E2, j < 16 ? g * 16 + j : f * 4 + i, (halves >> g) & 1u) + lane * 16;
#pragma unroll
      for (int k = 0; k < 4; ++k) dd[k] = __builtin_amdgcn_raw_buffer_load_b128(rw, o + k * 1024, 0, AUX_SC1);
      asm volatile("" :: "v"(dd[0]), "v"(dd[1]), "v"(dd[2]), "v"(dd[3]));
    }
  }
  unsigned hk[2] = {0u, 0u};
  const int ub = g * 14 + j;
  if (j < 14) {
    const int u = ub * 16 + 4 * lg + 2 * (li & 1);
    const u32x4 ph = gather1(ws, WS_H + (li >> 2) * 14336 + (u >> 6) * 512 + ((u >> 1) & 3) * 128 + ((u >> 3) & 7) * 16, tag);
    float h0 = __uint_as_float(ph[0]), h1 = __uint_as_float(ph[2]);
    h0 += dpp<ROR8>(h0); h1 += dpp<ROR8>(h1); h0 += dpp<ROR4>(h0); h1 += dpp<ROR4>(h1);
    const float hp = __uint_as_float(cvtpk(fmaxf(h0, 0.f), fmaxf(h1, 0.f)));
    hk[0] = __float_as_uint(dpp<0x00>(hp)); hk[1] = __float_as_uint(dpp<0x55>(hp));
  }
  if (hw == 2) {
    lds_wait<8>(sm_flag, 4 + (lane & 3));
    const int h = lane & 3;
    const float M = fmaxf(fmaxf(sm_m[h], sm_m[4 + h]), fmaxf(sm_m[8 + h], sm_m[12 + h])) * L2E;
    const u32x4* sp = reinterpret_cast<const u32x4*>(sm_s) + lg * 64 + (li >> 2) * 16 + h;
    float l = 0.f;
#pragma unroll 2
    for (int i = 0; i < NT; ++i) {
      const u32x4 sc = sp[i * 256];
#pragma unroll
      for (int k = 0; k < 4; ++k) l += __builtin_amdgcn_exp2f(__builtin_fmaf(bflo(sc[k]), L2E, -M)) + __builtin_amdgcn_exp2f(__builtin_fmaf(bfhi(sc[k]), L2E, -M));
    }
    l += dpp<ROR8>(l); l += dpp<ROR4>(l);
    const unsigned lse = __float_as_uint(M + __builtin_amdgcn_logf(xrow_sum(l)));
    if (lane < 8) __builtin_amdgcn_raw_buffer_store_b128(u32x4{lse, tag, lse, tag}, rw, lane * 16, WS_S + (g * 32 + j) * 128, AUX_SC1);
    __builtin_amdgcn_s_sleep(LSLEEP);
    u32x4 pl[4];
    gather4<8>(pl, ws, WS_S + g * 4096 + lane * 16, tag);
    float G = fmaxf(fmaxf(__uint_as_float(pl[0][0]), __uint_as_float(pl[1][0])), fmaxf(__uint_as_float(pl[2][0]), __uint_as_float(pl[3][0])));
    G = xrow_max(fmaxf(G, dpp<ROR8>(G)));
    float L = 0.f;
#pragma unroll
    for (int i = 0; i < 4; ++i) L += __builtin_amdgcn_exp2f(__uint_as_float(pl[i][0]) - G);
    L = xrow_sum(L + dpp<ROR8>(L));
    if (lane < 4) sm_lse[lane] = G + __builtin_amdgcn_logf(L);
    STAMP(6);
  }
  STAMP(4);
  if (j >= 18) {
    asm volatile("s_waitcnt lgkmcnt(0)" ::: "memory");
    STAMP_OUT(); VMDONE(); return;
  }
  if (j < 16) {
    u32x4 w2r[4], wor[8];
    const rsrc_t r2 = mkrsrc(w2, j < 14 ? DFF * DM * 2 : 0), ro = mkrsrc(wo, 4096 * DM * 2);
    BARRIER();
    STAMP(5);
#pragma unroll
    for (int i = 0; i < 4; ++i) w2r[i] = __builtin_amdgcn_raw_buffer_load_b128(r2, lg * 4096 + li * 16, (ub * 16 + i) * 1024 + hw * 256, AUX_NT);
#pragma unroll
    for (int i = 0; i < 8; ++i)
      wor[i] = __builtin_amdgcn_raw_buffer_load_b128(ro, lg * 2048 + li * 16, (g * 512 + j * 32 + 8 * (i & 3) + (i >> 2)) * 1024 + hw * 256, AUX_NT);
    const int fc = 4 * hw + lg;
    unsigned a_g = blk(WS_E2, fc * 4 + (g >> 1), (sm_w[2] >> (fc & 7)) & 1u) + ((g & 1) * 16 + j) * 128 + li * 8;
    unsigned a_l = (unsigned)(uintptr_t)(LAS float*)(sm_o + 4 * lg);
    PIN(a_g); PIN(a_l);
    BARRIER();
    float y[8] = {0, 0, 0, 0, 0, 0, 0, 0};
#pragma unroll
    for (int k = 0; k < 2; ++k)
#pragma unroll
      for (int c = 0; c < 8; ++c) y[c] = dot2(colpair(w2r[2 * k], w2r[2 * k + 1], c), hk[k], y[c]);
    unsigned wp[4][8];
#pragma unroll
    for (int k = 0; k < 4; ++k)
#pragma unroll
      for (int c = 0; c < 8; ++c) { wp[k][c] = colpair(wor[2 * k], wor[2 * k + 1], c); PIN(wp[k][c]); }
#pragma unroll
    for (int c = 0; c < 8; ++c) PIN(y[c]);
    STAMP(7);
    BARRIER();
    const u32x4 op = *reinterpret_cast<const LAS u32x4*>(a_l);
#pragma unroll
    for (int k = 0; k < 4; ++k)
#pragma unroll
      for (int c = 0; c < 8; ++c) y[c] = dot2(wp[k][c], op[k], y[c]);
    float c2[2];
    fold8(y, c2);
    __builtin_amdgcn_raw_buffer_store_b64(pack2(c2[0], c2[1], tag), rw, a_g, 0, AUX_SC1);
    STAMP(8);
  } else {
    const int fcol = 128 * (f >> 2) + 8 * li + 2 * (f & 3);
    const unsigned xo = __builtin_amdgcn_raw_buffer_load_b32(rx, fcol * 2, 0, 0);
    float x0 = bflo(xo), x1 = bfhi(xo);
    BARRIER();
    unsigned a_g = blk(WS_E2, f * 4 + hw, (sm_w[2] >> g) & 1u) + lane * 16, a_o = lane < 16 ? fcol * 2 : DM * 2;
    unsigned a_w = (unsigned)(uintptr_t)(LAS float*)(sm_o + hw * 32 + 4 * (lane & 7) + 2 * (lane >> 5)), a_r = (unsigned)(uintptr_t)(LAS float*)(sm_o + 2 * li);
    PIN(a_g); PIN(a_o); PIN(a_w); PIN(a_r); PIN(x0); PIN(x1);
    const rsrc_t rout = mkrsrc(out, hw == 0 ? DM * 2 : 0), rep = mkrsrc(ws, (hw == 1 && f == 0) ? 4 : 0);
    BARRIER();
    __builtin_amdgcn_s_sleep(E2DELAY);
    u32x4 pa[4];
    gather4<0>(pa, ws, a_g, tag);
    *reinterpret_cast<LAS f32x2*>(a_w) = sum_pieces(pa);
    BARRIER();
    STAMP(9);
    f32x2 r[4];
#pragma unroll
    for (int w = 0; w < 4; ++w) r[w] = *reinterpret_cast<const LAS f32x2*>(a_r + w * 128);
    const f32x2 t = (r[0] + r[1]) + (r[2] + r[3]);
    __builtin_amdgcn_raw_buffer_store_b32(cvtpk(x0 + t[0], x1 + t[1]), rout, a_o, 0, 0);
    __builtin_amdgcn_raw_buffer_store_b32(tag, rep, lane * 4, WS_EPOCH, AUX_SC1);
    STAMP(10);
  }
  STAMP_OUT();
  VMDONE();
}

extern "C" void kernel_launch(void* const* d_in, const int* in_sizes, int n_in, void* d_out, int out_size, void* d_ws, size_t ws_size,
                              hipStream_t stream) {
  fused_decode<<<256, 512, 0, stream>>>(d_in[1], d_in[2], d_ws, d_in[0], d_in[3], d_in[6], d_in[4], d_in[7], d_out);
}
```

```cpp
#include <hip/hip_runtime.h>
#include <hip/hip_bf16.h>
#include <stdint.h>

#define KNOB(name, val) constexpr int name = val;
#define WIN 6
#define WIN0 6
#define QPOS 26
#define QSLEEP 30
#define LSLEEP 110
#define E1DELAY 12
#define E2DELAY 40
#define CP 218
#define CT0 460
#define CG 8
#define CW0 11
#define CMIN 8
#define CMAX 16
#define GLOB 1
#define COFF 15
#define VSLEEP 90
#define LAYRT 0
#ifndef NVOTE0
#define NVOTE0 1
#endif
#ifndef NVOTE
#define NVOTE 3
#endif
#define FORCE -1
#define CODEPF_BYTES 21400
#define STAMPS 0

typedef unsigned short u16;
typedef unsigned long long u64;
typedef unsigned u32x4 __attribute__((ext_vector_type(4)));
typedef unsigned u32x2 __attribute__((ext_vector_type(2)));
typedef float f32x4 __attribute__((ext_vector_type(4)));
typedef float f32x2 __attribute__((ext_vector_type(2)));
typedef __bf16 bf16x8 __attribute__((ext_vector_type(8)));
typedef __bf16 bf16x2 __attribute__((ext_vector_type(2)));
typedef u64 u64x8 __attribute__((ext_vector_type(8)));
typedef __amdgpu_buffer_rsrc_t rsrc_t;
#define LAS __attribute__((address_space(3)))

constexpr int DM = 512, SK = 65536, DH = 128, DFF = 1792;
constexpr int NT = 16;
constexpr unsigned HEADB = SK * DH * 2;
constexpr unsigned WS_EPOCH = 0;
constexpr unsigned WS_V = 4096;
constexpr unsigned WS_Q = 16384;
constexpr unsigned WS_H = WS_Q + 8 * 16384;
constexpr unsigned WS_S = WS_H + 65536;
constexpr unsigned WS_P = WS_S + 8 * 4096;
constexpr unsigned WS_E1 = WS_P + 8 * 16384;
constexpr unsigned WS_E2 = WS_E1 + 64 * 16384;
constexpr unsigned WS_END = WS_E2 + 32 * 16384;
static_assert(WS_P % 16384 == 0 && WS_END <= (2u << 20), "layout");
__device__ __forceinline__ unsigned blk(unsigned base, unsigned i, unsigned half) { return base + (i >> 1) * 16384 + (i & 1) * 4096 + half * 8192; }
constexpr unsigned WS_STAMP = 16u << 20;
constexpr int ACCL = 36, ACCH = 4 * ACCL + 4;
constexpr int SPIN_CAP = 1 << 18;
constexpr int AUX_NT = 2, AUX_SC1 = 16;
constexpr float L2E = 1.4426950408889634f, SCALE = 0.088388347648318440f;

#define LAY "jjjwwnttnjtjnn"
constexpr unsigned lay_vec(int i) {
  constexpr int cb[7] = {8, 9, 10, 11, 14, 12, 13};
  return LAY[14] == '_' ? 1u << (10 + i) : i < 5 ? 1u << cb[i + 2] : ((1u << (10 + i)) | (1u << cb[(i - 5) % 7])) & ~1023u;
}
#define DEP(C, O, v) deposit<C>(v)
template <char C> __device__ __forceinline__ unsigned deposit(unsigned v) {
  unsigned r = 0; int k = 0;
#pragma unroll
  for (int i = 0; i < 14; ++i) if (LAY[i] == C) { r ^= ((v >> k) & 1u) * lay_vec(i); ++k; }
  return r;
}
__device__ __forceinline__ float bflo(unsigned v) { return __uint_as_float(v << 16); }
__device__ __forceinline__ float bfhi(unsigned v) { return __uint_as_float(v & 0xffff0000u); }
__device__ __forceinline__ unsigned cvtpk(float lo, float hi) {
  bf16x2 r; r[0] = (__bf16)lo; r[1] = (__bf16)hi; return __builtin_bit_cast(unsigned, r);
}
__device__ __forceinline__ float rbf(float f) { return bflo(cvtpk(f, f)); }
template <int DC> __device__ __forceinline__ float dpp(float x) {
  return __builtin_bit_cast(float, __builtin_amdgcn_mov_dpp(__builtin_bit_cast(int, x), DC, 0xf, 0xf, true));
}
constexpr int ROR1 = 0x121, ROR2 = 0x122, ROR4 = 0x124, ROR8 = 0x128, SHL4 = 0x104, SHL8 = 0x108, SHL12 = 0x10c;
__device__ __forceinline__ float xrow_max(float x) {
  auto s = __builtin_amdgcn_permlane16_swap(__float_as_uint(x), __float_as_uint(x), false, false);
  x = fmaxf(__uint_as_float(s[0]), __uint_as_float(s[1]));
  auto t = __builtin_amdgcn_permlane32_swap(__float_as_uint(x), __float_as_uint(x), false, false);
  return fmaxf(__uint_as_float(t[0]), __uint_as_float(t[1]));
}
__device__ __forceinline__ float xrow_sum(float x) {
  auto s = __builtin_amdgcn_permlane16_swap(__float_as_uint(x), __float_as_uint(x), false, false);
  x = __uint_as_float(s[0]) + __uint_as_float(s[1]);
  auto t = __builtin_amdgcn_permlane32_swap(__float_as_uint(x), __float_as_uint(x), false, false);
  return __uint_as_float(t[0]) + __uint_as_float(t[1]);
}
__device__ __forceinline__ float row_sum(float x) {
  x += dpp<ROR8>(x); x += dpp<ROR4>(x); x += dpp<ROR2>(x); x += dpp<ROR1>(x); return x;
}
__device__ __forceinline__ float wave_sum(float x) { return xrow_sum(row_sum(x)); }
__device__ __forceinline__ float dot2(unsigned a, unsigned b, float c) {
  return __builtin_amdgcn_fdot2_f32_bf16(__builtin_bit_cast(bf16x2, a), __builtin_bit_cast(bf16x2, b), c, false);
}
__device__ __forceinline__ unsigned perm_lo(unsigned hi, unsigned lo) { return __builtin_amdgcn_perm(hi, lo, 0x05040100u); }
__device__ __forceinline__ unsigned perm_hi(unsigned hi, unsigned lo) { return __builtin_amdgcn_perm(hi, lo, 0x07060302u); }
__device__ __forceinline__ unsigned colpair(const u32x4& r0, const u32x4& r1, int c) {
  return (c & 1) ? perm_hi(r1[c >> 1], r0[c >> 1]) : perm_lo(r1[c >> 1], r0[c >> 1]);
}
__device__ __forceinline__ rsrc_t mkrsrc(const void* p, unsigned bytes) { return __builtin_amdgcn_make_buffer_rsrc(const_cast<void*>(p), 0, bytes, 0x00020000); }
__device__ __forceinline__ f32x4 mfma(u32x4 a, u32x4 b, f32x4 c) {
  return __builtin_amdgcn_mfma_f32_16x16x32_bf16(__builtin_bit_cast(bf16x8, a), __builtin_bit_cast(bf16x8, b), c, 0, 0, 0);
}
__device__ __forceinline__ void fold8(const float (&a)[8], float (&c2)[2]) {
  float b4[4];
#pragma unroll
  for (int k = 0; k < 4; ++k) { auto r = __builtin_amdgcn_permlane32_swap(__float_as_uint(a[k]), __float_as_uint(a[k + 4]), false, false); b4[k] = __uint_as_float(r[0]) + __uint_as_float(r[1]); }
#pragma unroll
  for (int k = 0; k < 2; ++k) { auto r = __builtin_amdgcn_permlane16_swap(__float_as_uint(b4[k]), __float_as_uint(b4[k + 2]), false, false); c2[k] = __uint_as_float(r[0]) + __uint_as_float(r[1]); }
}

__device__ __forceinline__ u32x2 pack2(float a, float b, unsigned tag) {
  return u32x2{__builtin_amdgcn_perm(__float_as_uint(a), __float_as_uint(b), 0x07060503u), __builtin_amdgcn_perm(__float_as_uint(b), tag, 0x06050100u)};
}
__device__ __forceinline__ float unpackA(unsigned w0) { return __uint_as_float(w0 & 0xffffff00u); }
__device__ __forceinline__ float unpackB(unsigned w0, unsigned w1) { return __uint_as_float(__builtin_amdgcn_perm(w0, w1, 0x0403020cu)); }
__device__ __forceinline__ f32x2 sum_pieces(const u32x4 (&p)[4]) {
  float v[4];
  v[0] = (unpackA(p[0][0]) + unpackA(p[1][0])) + (unpackA(p[2][0]) + unpackA(p[3][0]));
  v[1] = (unpackB(p[0][0], p[0][1]) + unpackB(p[1][0], p[1][1])) + (unpackB(p[2][0], p[2][1]) + unpackB(p[3][0], p[3][1]));
  v[2] = (unpackA(p[0][2]) + unpackA(p[1][2])) + (unpackA(p[2][2]) + unpackA(p[3][2]));
  v[3] = (unpackB(p[0][2], p[0][3]) + unpackB(p[1][2], p[1][3])) + (unpackB(p[2][2], p[2][3]) + unpackB(p[3][2], p[3][3]));
  f32x2 r;
#pragma unroll
  for (int k = 0; k < 2; ++k) {
    auto t = __builtin_amdgcn_permlane32_swap(__float_as_uint(v[k]), __float_as_uint(v[k + 2]), false, false);
    float x = __uint_as_float(t[0]) + __uint_as_float(t[1]);
    x += dpp<ROR8>(x);
    auto u = __builtin_amdgcn_permlane16_swap(__float_as_uint(x), __float_as_uint(x), false, false);
    r[k] = __uint_as_float(u[0]) + __uint_as_float(u[1]);
  }
  return r;
}

template <int SLP>
__device__ __forceinline__ void gather4(u32x4 (&a)[4], const char* base, unsigned off, unsigned tag) {
  u64 m0, m1, m2, m3, t0; int cnt = SPIN_CAP;
  asm volatile(
      "s_mov_b64 %[m0], -1\n\ts_mov_b64 %[m1], -1\n\ts_mov_b64 %[m2], -1\n\ts_mov_b64 %[m3], -1\n\ts_nop 0\n"
      "0:\n\t"
      "s_wqm_b64 exec, %[m0]\n\tglobal_load_dwordx4 v[40:43], %[off], %[base] sc1\n\t"
      "s_wqm_b64 exec, %[m1]\n\tglobal_load_dwordx4 v[44:47], %[off], %[base] offset:1024 sc1\n\t"
      "s_wqm_b64 exec, %[m2]\n\tglobal_load_dwordx4 v[48:51], %[off], %[base] offset:2048 sc1\n\t"
      "s_wqm_b64 exec, %[m3]\n\tglobal_load_dwordx4 v[52:55], %[off], %[base] offset:3072 sc1\n\t"
      "s_mov_b64 exec, -1\n\t"
      "s_sub_u32 %[cnt], %[cnt], 1\n\ts_cbranch_scc1 9f\n\t"
      "s_waitcnt vmcnt(0)\n\t"
      "v_bitop3_b32 v56, v41, v43, %[tag] bitop3:0x7e\n\tv_bitop3_b32 v57, v45, v47, %[tag] bitop3:0x7e\n\t"
      "v_bitop3_b32 v58, v49, v51, %[tag] bitop3:0x7e\n\tv_bitop3_b32 v59, v53, v55, %[tag] bitop3:0x7e\n\t"
      "v_cmp_ne_u16 %[m0], 0, v56\n\tv_cmp_ne_u16 %[m1], 0, v57\n\tv_cmp_ne_u16 %[m2], 0, v58\n\tv_cmp_ne_u16 %[m3], 0, v59\n\t"
      ".if %c[slp]\n\ts_sleep %c[slp]\n\t.endif\n\t"
      "s_or_b64 %[t0], %[m0], %[m1]\n\ts_or_b64 vcc, %[m2], %[m3]\n\ts_or_b64 %[t0], %[t0], vcc\n\t"
      "s_cbranch_scc1 0b\n"
      "9:\n\ts_waitcnt vmcnt(0)"
      : "=&{v[40:43]}"(a[0]), "=&{v[44:47]}"(a[1]), "=&{v[48:51]}"(a[2]), "=&{v[52:55]}"(a[3]),
        [m0] "=&s"(m0), [m1] "=&s"(m1), [m2] "=&s"(m2), [m3] "=&s"(m3), [t0] "=&s"(t0), [cnt] "+s"(cnt)
      : [tag] "s"(tag), [base] "s"(base), [off] "v"(off), [slp] "i"(SLP)
      : "memory", "vcc", "scc", "v56", "v57", "v58", "v59");
}
__device__ __forceinline__ u32x4 load_dev(const char* base, unsigned off) {
  u32x4 a;
  asm volatile("global_load_dwordx4 %0, %1, %2 sc1\n\ts_waitcnt vmcnt(0)" : "=&v"(a) : "v"(off), "s"(base) : "memory");
  return a;
}
__device__ __forceinline__ u32x4 gather1(const char* base, unsigned off, unsigned tag) {
  u32x4 p;
#pragma unroll 1
  for (int spin = 0; spin < SPIN_CAP; ++spin) {
    p = load_dev(base, off);
    if (__builtin_amdgcn_ballot_w64(((p[1] ^ tag) | (p[3] ^ tag)) != 0) == 0) break;
    __builtin_amdgcn_s_sleep(32);
  }
  return p;
}
template <int SLP>
__device__ __forceinline__ void lds_wait(const unsigned* flags, int idx) {
#pragma unroll 1
  for (int spin = 0; spin < SPIN_CAP; ++spin) {
    if (__builtin_amdgcn_ballot_w64(*(volatile LAS unsigned*)(flags + idx) == 0u) == 0) break;
    __builtin_amdgcn_s_sleep(SLP);
  }
  asm volatile("" ::: "memory");
}
__device__ __forceinline__ void lds_set(unsigned* flag) {
  asm volatile("" ::: "memory");
  *(volatile LAS unsigned*)flag = 1u;
}
__device__ __forceinline__ int timed_read(const char* p) {
  u64 t0, t1; unsigned d;
  asm volatile("s_memtime %0\n\ts_waitcnt lgkmcnt(0)\n\ts_load_dword %2, %3, 0\n\ts_waitcnt lgkmcnt(0)\n\ts_memtime %1\n\ts_waitcnt lgkmcnt(0)"
               : "=&s"(t0), "=&s"(t1), "=&s"(d) : "s"(p) : "memory");
  return (int)((unsigned)t1 - (unsigned)t0);
}

#define SB() __builtin_amdgcn_sched_barrier(0)
#define PIN(x) asm volatile("" : "+v"(x))
#define PACE() do { SB(); asm volatile("s_waitcnt vmcnt(%0)" :: "i"(WIN - 1) : "memory"); } while (0)
#define PACEV(t) do { SB(); asm volatile("s_swappc_b64 s[96:97], %0" :: "s"(((t) & 1) ? tgtB : tgtA) : "memory", "s96", "s97"); } while (0)
#define VMDONE() __builtin_amdgcn_s_waitcnt(0x0f70)
#define BARRIER() do { SB(); asm volatile("s_waitcnt lgkmcnt(0)\n\ts_barrier" ::: "memory"); SB(); } while (0)
#define STAMP(i)
#define PROG(i)
#define STAMP_OUT()

__global__ void __launch_bounds__(512, 2) __attribute__((aligned(4096)))
fused_decode(const void* K_, const void* V_, void* ws_, const void* x_, const void* wq_, const void* w1_, const void* wo_, const void* w2_, void* out_) {
  __shared__ __attribute__((aligned(16))) float lds[2048 + 16 * ACCH + 128 + 64 + 4 * NT * 256];
  float* const sm_q = lds;
  float* const sm_acc = lds + 2048;
  float* const sm_o = sm_acc + 16 * ACCH;
  float* const sm_m = sm_o + 128;
  float* const sm_lse = sm_m + 16;
  unsigned* const sm_w = reinterpret_cast<unsigned*>(sm_lse + 4);
  unsigned* const sm_flag = sm_w + 12;
  float* const sm_s = sm_m + 64;
  asm volatile("s_load_dwordx16 s[64:79], %0, 0\n\ts_load_dwordx2 s[80:81], %0, 0x40" :: "s"(__builtin_amdgcn_kernarg_segment_ptr())
               : "s64", "s65", "s66", "s67", "s68", "s69", "s70", "s71", "s72", "s73", "s74", "s75", "s76", "s77", "s78", "s79", "s80", "s81");
  SB();
  const int b = blockIdx.x, g = b & 7, j = b >> 3;
  const int tid = threadIdx.x, lane = tid & 63, wave = __builtin_amdgcn_readfirstlane(tid >> 6);
  const int lg = lane >> 4, li = lane & 15;
  unsigned koff = (li >> 2) * 256 + lg * 64 + (li & 3) * 16, voff = lane * 16;
  const unsigned wbase = deposit<'j'>(j) ^ deposit<'w'>(wave);
  unsigned qoff = (lane >> 3) * 16 * 8192 + (lane & 7) * 16, xoff = (lane >> 3) * 32;
  if (tid < 16) sm_flag[tid] = 0u;
  u64x8 ka; u64 ka8;
  {
    u64 pc;
    asm volatile("s_cmp_lg_u32 %8, 7\n\ts_cbranch_scc1 1f\n\ts_getpc_b64 %2\n\ts_lshr_b64 %2, %2, 12\n\ts_lshl_b64 %2, %2, 12\n\tglobal_load_dword v255, %7, %2\n1:\n\t"
                 "s_waitcnt lgkmcnt(0)"
                 : "={s[64:79]}"(ka), "={s[80:81]}"(ka8), "=&s"(pc), "+v"(koff), "+v"(voff), "+v"(qoff), "+v"(xoff)
                 : "v"(min(j * 1024 + (lane & 7) * 128, CODEPF_BYTES - 4)), "s"(wave) : "memory", "scc", "v255");
  }
  STAMP(12);
  const char *K = (const char*)ka[0], *V = (const char*)ka[1], *x = (const char*)ka[3], *wq = (const char*)ka[4], *w1 = (const char*)ka[5], *wo = (const char*)ka[6], *w2 = (const char*)ka[7];
  char *ws = (char*)((ka[2] + 0x1fffffull) & ~0x1fffffull), *out = (char*)ka8;
  const int hw = wave - 4;
  const rsrc_t rx = mkrsrc(x, DM * 2);
  const rsrc_t rw = mkrsrc(ws, WS_END);
  unsigned tag = 0;

  u32x4 xr, xa, xb, wr[16];
  auto proj_load = [&](const char* W, unsigned ldb, unsigned woff, int rb, unsigned colb, bool paced) {
    xr = __builtin_amdgcn_raw_buffer_load_b128(rx, voff, 0, 0);
    xa = __builtin_amdgcn_raw_buffer_load_b128(rx, xoff, rb * 256, 0); xb = __builtin_amdgcn_raw_buffer_load_b128(rx, xoff + 16, rb * 256, 0);
    const rsrc_t rm = mkrsrc(W, DM * ldb);
#pragma unroll
    for (int i = 0; i < 16; ++i) {
      wr[i] = __builtin_amdgcn_raw_buffer_load_b128(rm, woff, (128 * rb + i) * ldb + colb, AUX_NT);
      if (paced && (i & 1)) { SB(); asm volatile("s_waitcnt vmcnt(2)" ::: "memory"); SB(); }
    }
  };
  auto proj_send = [&](unsigned dst) {
    float xf[8];
#pragma unroll
    for (int k = 0; k < 4; ++k) { xf[2 * k] = bflo(xr[k]); xf[2 * k + 1] = bfhi(xr[k]); }
    const float mean = wave_sum(((xf[0] + xf[1]) + (xf[2] + xf[3])) + ((xf[4] + xf[5]) + (xf[6] + xf[7]))) * (1.f / 512.f);
    float vs = 0.f;
#pragma unroll
    for (int k = 0; k < 8; ++k) { const float d = xf[k] - mean; vs += d * d; }
    const float var = wave_sum(vs) * (1.f / 512.f);
    const float mb = rbf(mean), rden = __builtin_amdgcn_rcpf(rbf(__builtin_amdgcn_sqrtf(rbf(rbf(var) + 1e-5f))));
    auto ln2 = [&](unsigned xp) -> unsigned { return cvtpk(rbf(bflo(xp) - mb) * rden, rbf(bfhi(xp) - mb) * rden); };
    unsigned xn[8];
#pragma unroll
    for (int k = 0; k < 4; ++k) { xn[k] = ln2(xa[k]); xn[4 + k] = ln2(xb[k]); }
    float a[8] = {0, 0, 0, 0, 0, 0, 0, 0};
#pragma unroll
    for (int k = 0; k < 8; ++k)
#pragma unroll
      for (int c = 0; c < 8; ++c) a[c] = dot2(colpair(wr[2 * k], wr[2 * k + 1], c), xn[k], a[c]);
    float c2[2];
    fold8(a, c2);
    c2[0] += dpp<ROR8>(c2[0]); c2[1] += dpp<ROR8>(c2[1]);
    if (li < 8) __builtin_amdgcn_raw_buffer_store_b128(u32x4{__float_as_uint(c2[0]), tag, __float_as_uint(c2[1]), tag}, rw, lg * 128 + li * 16, dst, AUX_SC1);
  };
  unsigned epoch;
  if (wave == 4) {
    epoch = __builtin_amdgcn_raw_buffer_load_b32(rw, 0, WS_EPOCH, AUX_SC1);
    proj_load(wq, 8192u, qoff, j >> 3, g * 1024 + (j & 7) * 128, false);
  }
  SB();
  asm volatile("s_barrier" ::: "memory");
  SB();

  const rsrc_t rk = mkrsrc(K + (size_t)g * HEADB, HEADB), rv = mkrsrc(V + (size_t)g * HEADB, HEADB);
  auto toff = [&](int n) -> unsigned { return wbase ^ DEP('n', 7, n); };
  auto ldk = [&](unsigned so, int t) -> u32x4 { return __builtin_amdgcn_raw_buffer_load_b128(rk, koff, so ^ DEP('t', 11, t), AUX_NT); };
  auto ldv = [&](unsigned so, int t) -> u32x4 { return __builtin_amdgcn_raw_buffer_load_b128(rv, voff, so ^ DEP('t', 11, t), AUX_NT); };
  u32x4 ring[4][8];
  int role = wave >> 2;
  asm volatile("" : "+s"(role));
  if (role == 0) {
#pragma unroll
    for (int i = 0; i < QPOS; ++i) {
      if (i == WIN0 && WIN0 < WIN) { SB(); asm volatile("s_waitcnt vmcnt(%0)" :: "i"(WIN0 - 1) : "memory"); }
      if (i >= WIN) PACE();
      ring[i >> 3][i & 7] = ldk(toff(i >> 3), i & 7);
      SB();
      if (i == 7) STAMP(1);
    }
    STAMP(2);
    lds_wait<1>(sm_flag, lane & 3);
    STAMP(3);
    u32x4 qb;
    {
      const float* qp = sm_q + (li & 3) * 128 + 8 * (4 * lg + (li >> 2));
      f32x4 a = *reinterpret_cast<const f32x4*>(qp), c = *reinterpret_cast<const f32x4*>(qp + 4);
#pragma unroll
      for (int r = 1; r < 4; ++r) { a += *reinterpret_cast<const f32x4*>(qp + 512 * r); c += *reinterpret_cast<const f32x4*>(qp + 512 * r + 4); }
      qb = u32x4{cvtpk(a[0], a[1]), cvtpk(a[2], a[3]), cvtpk(c[0], c[1]), cvtpk(c[2], c[3])};
    }
    tag = __builtin_amdgcn_readfirstlane(sm_w[0]);
#pragma unroll
    for (int i = QPOS; i < 32; ++i) { PACE(); ring[i >> 3][i & 7] = ldk(toff(i >> 3), i & 7); SB(); }
    u64 tbl, tgtA, tgtB;
    asm volatile("s_getpc_b64 %0\n\ts_branch 1f\n\t.set w, 0\n\t.rept 16\n\ts_waitcnt vmcnt(w)\n\ts_setpc_b64 s[96:97]\n\t.set w, w+1\n\t.endr\n1:" : "=s"(tbl));
    auto steer = [&](int n) {
      const unsigned now = (unsigned)__builtin_amdgcn_s_memrealtime();
      const int lag = (int)(now - __builtin_amdgcn_readfirstlane(*(volatile LAS unsigned*)(sm_w + 1))) - ((n * CP) >> 1);
      const int w2 = min(max(CW0 + ((lag * CG) >> 8), CMIN), CMAX);
      tgtA = tbl + (unsigned)(8 * (w2 >> 1) - 4); tgtB = tbl + (unsigned)(8 * ((w2 + 1) >> 1) - 4);
    };
    *(volatile LAS unsigned*)(sm_flag + 12 + wave) = (unsigned)__builtin_amdgcn_s_memrealtime() | 1u;
    u32x4* const sp = reinterpret_cast<u32x4*>(sm_s) + wave * (NT * 64) + lane;
    float mraw = -INFINITY;
#pragma unroll 1
    for (int it = 0; it < NT; it += 4) {
      const bool last = it == NT - 4;
      const rsrc_t rn = mkrsrc(last ? V + (size_t)g * HEADB : K + (size_t)g * HEADB, HEADB);
      const unsigned noff = last ? voff : koff;
#pragma unroll
      for (int u = 0; u < 4; ++u) {
        if (!(u & 1)) steer(it + u);
        const unsigned so = toff((it + u + 4) & (NT - 1));
        u32x4 sc;
        float v[2];
#pragma unroll
        for (int t = 0; t < 8; ++t) {
          const f32x4 d = mfma(ring[u][t], qb, f32x4{0, 0, 0, 0});
          PACEV(t);
          ring[u][t] = __builtin_amdgcn_raw_buffer_load_b128(rn, noff, so ^ DEP('t', 11, t), AUX_NT);
          SB();
          v[t & 1] = ((d[0] + dpp<SHL4>(d[1])) + dpp<SHL8>(d[2])) + dpp<SHL12>(d[3]);
          if (t & 1) {
            const unsigned r = cvtpk(v[0], v[1]);
            sc[t >> 1] = cvtpk(bflo(r) * SCALE, bfhi(r) * SCALE);
            mraw = fmaxf(mraw, fmaxf(v[0], v[1]));
          }
        }
        sp[(it + u) * 64] = sc;
        if (u & 1) PROG((it + u) >> 1);
      }
    }
    const float m = rbf(rbf(xrow_max(mraw)) * SCALE);
    float ml2 = m * L2E;
    if (lane < 4) sm_m[wave * 4 + lane] = m;
    lds_set(sm_flag + 4 + wave);
    f32x4 acc[8];
#pragma unroll
    for (int e = 0; e < 8; ++e) acc[e] = f32x4{0, 0, 0, 0};
    auto probs = [&](unsigned sc) -> unsigned {
      return cvtpk(__builtin_amdgcn_exp2f(__builtin_fmaf(bflo(sc), L2E, -ml2)), __builtin_amdgcn_exp2f(__builtin_fmaf(bfhi(sc), L2E, -ml2)));
    };
    auto pv = [&](const u32x4 (&vc)[8], u32x4 (&vn)[8], int n, auto refill) {
      constexpr bool R = decltype(refill)::value;
      const unsigned so = toff(n + 3);
#define VSTEP(t) do { if constexpr (R) { PACEV(t); vn[t] = ldv(so, t); SB(); } } while (0)
      const u32x4 sc = sp[n * 64];
      VSTEP(0);
      u32x4 pb;
#pragma unroll
      for (int k = 0; k < 4; ++k) {
        pb[k] = probs(sc[k]);
        if (k & 1) VSTEP(1 + (k >> 1));
      }
      VSTEP(3);
#pragma unroll
      for (int e = 0; e < 8; ++e) {
        u32x4 f;
#pragma unroll
        for (int k = 0; k < 4; ++k) f[k] = colpair(vc[2 * k], vc[2 * k + 1], e);
        acc[e] = mfma(f, pb, acc[e]);
        if (e & 1) VSTEP(4 + (e >> 1));
      }
#undef VSTEP
    };
    using T = std::integral_constant<bool, true>;
    using F = std::integral_constant<bool, false>;
    pv(ring[0], ring[3], 0, F{});
#pragma unroll 1
    for (int it = 1; it < NT - 3; it += 4) {
      steer(15 + it);
      pv(ring[1], ring[0], it, T{});
      pv(ring[2], ring[1], it + 1, T{});
      PROG(8 + (it >> 1));
      steer(17 + it);
      pv(ring[3], ring[2], it + 2, T{});
      pv(ring[0], ring[3], it + 3, T{});
      PROG(9 + (it >> 1));
    }
    STAMP(4);
    BARRIER();
    {
      const float lse = sm_lse[li & 3], fs = __builtin_amdgcn_exp2f(ml2 - lse);
      ml2 = lse;
#pragma unroll
      for (int e = 0; e < 8; ++e) acc[e] *= fs;
    }
    const unsigned half = (sm_w[2] >> g) & 1u;
    unsigned a_wr = (unsigned)(uintptr_t)(LAS float*)(sm_acc + (wave * 4 + (li & 3)) * ACCH + lg * ACCL);
    unsigned a_l2 = (unsigned)(uintptr_t)(LAS float*)(sm_acc + (2 * wave + (lane >> 5)) * ACCH + ((lane >> 3) & 3) * ACCL + 4 * (lane & 7));
    unsigned a_l3 = (unsigned)(uintptr_t)(LAS float*)(sm_o + 2 * (lane & 7) + (lane >> 5));
    unsigned a_g = blk(WS_E1, g * 16 + wave * 8 + (lane >> 3), half) + j * 128 + (lane & 7) * 16, a_in = blk(WS_E1, g * 16 + (j & 15), half) + lane * 16;
    PIN(a_wr); PIN(a_l2); PIN(a_l3); PIN(a_g); PIN(a_in);
    pv(ring[1], ring[0], NT - 3, F{});
    pv(ring[2], ring[0], NT - 2, F{});
    {
      const u32x4 sc = sp[(NT - 1) * 64];
      u32x4 pb, f[8];
#pragma unroll
      for (int k = 0; k < 4; ++k) pb[k] = probs(sc[k]);
#pragma unroll
      for (int k = 0; k < 3; ++k) {
        SB();
#pragma unroll
        for (int e = 0; e < 8; ++e) f[e][k] = colpair(ring[3][2 * k], ring[3][2 * k + 1], e);
      }
      SB();
#pragma unroll
      for (int e = 0; e < 8; ++e) { f[e][3] = colpair(ring[3][6], ring[3][7], e); acc[e] = mfma(f[e], pb, acc[e]); }
    }
    STAMP(5);
    if (li < 4) {
#pragma unroll
      for (int e = 0; e < 8; ++e) *reinterpret_cast<LAS f32x4*>(a_wr + 16 * e) = acc[e];
    }
    BARRIER();
    STAMP(6);
    if (wave < 2) {
      const f32x4 v = (*reinterpret_cast<const LAS f32x4*>(a_l2) + *reinterpret_cast<const LAS f32x4*>(a_l2 + 16 * ACCH)) +
                      (*reinterpret_cast<const LAS f32x4*>(a_l2 + 32 * ACCH) + *reinterpret_cast<const LAS f32x4*>(a_l2 + 48 * ACCH));
      const u32x2 g0 = pack2(v[0], v[1], tag), g1 = pack2(v[2], v[3], tag);
      __builtin_amdgcn_raw_buffer_store_b128(u32x4{g0[0], g0[1], g1[0], g1[1]}, rw, a_g, 0, AUX_SC1);
      STAMP(7);
    } else if (wave == 2 && j < 16) {
      __builtin_amdgcn_s_sleep(E1DELAY);
      u32x4 pa[4];
      gather4<0>(pa, ws, a_in, tag);
      STAMP(8);
      const f32x2 o = sum_pieces(pa);
      *reinterpret_cast<LAS unsigned*>(a_l3) = cvtpk(o[0], o[1]);
    }
    BARRIER();
    STAMP_OUT();
    asm volatile("s_endpgm" : "=v"(wr[0]), "=v"(wr[1]), "=v"(wr[2]), "=v"(wr[3]), "=v"(wr[4]), "=v"(wr[5]), "=v"(wr[6]), "=v"(wr[7]), "=v"(wr[8]), "=v"(wr[9]),
                 "=v"(wr[10]), "=v"(wr[11]), "=v"(wr[12]), "=v"(wr[13]), "=v"(wr[14]), "=v"(wr[15]), "=v"(xr), "=v"(xa), "=v"(xb), "=v"(epoch) :: "memory");
  }

  if (wave == 4) {
    tag = __builtin_amdgcn_readfirstlane(epoch) + 1u;
    SB();
    const unsigned t0 = (unsigned)__builtin_amdgcn_s_memrealtime();
    *reinterpret_cast<u32x2*>(sm_w) = u32x2{tag, t0 + CT0};
    STAMP(1);
    proj_send(WS_Q + g * 16384 + (j >> 3) * 4096 + (j & 7) * 512);
    lds_set(sm_flag + 8);
    STAMP(2);
  }
  int vote = 0;
  auto sample = [&](int k) {
    const char* p = ws + WS_P + k * 16384 + (((j - 24 + 4 * k) & 7) * 8 + g) * 128;
    vote += min(max(timed_read(p) - timed_read(p + 8192), -600), 600);
  };
  if (hw == 3 && j >= 24) {
    timed_read(ws + WS_P + 7 * 16384);
#pragma unroll 1
    for (int k = 0; k < NVOTE0; ++k) sample(k);
  }
  lds_wait<1>(sm_flag, 8);
  tag = __builtin_amdgcn_readfirstlane(sm_w[0]);
  __builtin_amdgcn_s_sleep(QSLEEP);
  {
    u32x4 pq[4];
    gather4<0>(pq, ws, WS_Q + g * 16384 + hw * 4096 + lane * 16, tag);
#pragma unroll
    for (int i = 0; i < 4; ++i) *reinterpret_cast<u32x2*>(sm_q + hw * 512 + i * 128 + (lane >> 5) * 64 + (lane & 7) * 8 + ((lane >> 3) & 3) * 2) = u32x2{pq[i][0], pq[i][2]};
    lds_set(sm_flag + hw);
    STAMP(11);
  }
  if (hw == 1 && j >= 18) {
    const int item = g * 14 + j - 18;
    proj_load(w1, DFF * 2, (lane >> 3) * 16 * (DFF * 2) + (lane & 7) * 16, item & 3, (item >> 2) * 128, true);
    proj_send(WS_H + (item & 3) * 14336 + (item >> 2) * 512);
  }
  const int f = (j - 16) * 8 + g;
  if (hw == 3) {
    if (j >= 24) {
#pragma unroll 1
      for (int k = NVOTE0; k < NVOTE; ++k) sample(k);
    }
    int d = vote;
    lds_wait<4>(sm_flag, 12);
    const unsigned t1 = sm_flag[12];
    if (lane < 4 && j >= 24) __builtin_amdgcn_raw_buffer_store_b128(u32x4{(unsigned)d, tag, t1, tag}, rw, lane * 16, WS_V + (g * 8 + j - 24) * 128, AUX_SC1);
    __builtin_amdgcn_s_sleep(VSLEEP);
    const u32x4 vt = gather1(ws, WS_V + lane * 128, tag);
    if (GLOB) *(volatile LAS unsigned*)(sm_w + 1) = t1 + COFF + (int)(wave_sum((float)(int)(vt[2] - t1)) * (1.f / 64.f));
    d = (int)vt[0];
    d += __builtin_amdgcn_mov_dpp(d, 0xB1, 0xf, 0xf, true); d += __builtin_amdgcn_mov_dpp(d, 0x4E, 0xf, 0xf, true); d += __builtin_amdgcn_mov_dpp(d, 0x141, 0xf, 0xf, true);
    unsigned halves = (unsigned)(((__builtin_amdgcn_ballot_w64(d > 0) & 0x0101010101010101ull) * 0x0102040810204080ull) >> 56);
    if (FORCE >= 0) halves = FORCE;
    sm_w[2] = halves;
    const int nb = j < 16 ? 1 : j < 18 ? 4 : 0;
#pragma unroll 1
    for (int i = 0; i < nb; ++i) {
      u32x4 dd[4];
      const unsigned o = blk(j < 16 ? WS_E1 : WS_E2, j < 16 ? g * 16 + j : f * 4 + i, (halves >> g) & 1u) + lane * 16;
#pragma unroll
      for (int k = 0; k < 4; ++k) dd[k] = __builtin_amdgcn_raw_buffer_load_b128(rw, o + k * 1024, 0, AUX_SC1);
      asm volatile("" :: "v"(dd[0]), "v"(dd[1]), "v"(dd[2]), "v"(dd[3]));
    }
  }
  unsigned hk[2] = {0u, 0u};
  const int ub = g * 14 + j;
  if (j < 14) {
    for (int q = hw == 3 ? 5 : 0; q < 6; ++q) __builtin_amdgcn_s_sleep(64);
    const int u = ub * 16 + 4 * lg + 2 * (li & 1);
    const u32x4 ph = gather1(ws, WS_H + (li >> 2) * 14336 + (u >> 6) * 512 + ((u >> 1) & 3) * 128 + ((u >> 3) & 7) * 16, tag);
    float h0 = __uint_as_float(ph[0]), h1 = __uint_as_float(ph[2]);
    h0 += dpp<ROR8>(h0); h1 += dpp<ROR8>(h1); h0 += dpp<ROR4>(h0); h1 += dpp<ROR4>(h1);
    const float hp = __uint_as_float(cvtpk(fmaxf(h0, 0.f), fmaxf(h1, 0.f)));
    hk[0] = __float_as_uint(dpp<0x00>(hp)); hk[1] = __float_as_uint(dpp<0x55>(hp));
  }
  if (hw == 2) {
    lds_wait<8>(sm_flag, 4 + (lane & 3));
    const int h = lane & 3;
    const float M = fmaxf(fmaxf(sm_m[h], sm_m[4 + h]), fmaxf(sm_m[8 + h], sm_m[12 + h])) * L2E;
    const u32x4* sp = reinterpret_cast<const u32x4*>(sm_s) + lg * 64 + (li >> 2) * 16 + h;
    float l = 0.f;
#pragma unroll 2
    for (int i = 0; i < NT; ++i) {
      const u32x4 sc = sp[i * 256];
#pragma unroll
      for (int k = 0; k < 4; ++k) l += __builtin_amdgcn_exp2f(__builtin_fmaf(bflo(sc[k]), L2E, -M)) + __builtin_amdgcn_exp2f(__builtin_fmaf(bfhi(sc[k]), L2E, -M));
    }
    l += dpp<ROR8>(l); l += dpp<ROR4>(l);
    const unsigned lse = __float_as_uint(M + __builtin_amdgcn_logf(xrow_sum(l)));
    if (lane < 8) __builtin_amdgcn_raw_buffer_store_b128(u32x4{lse, tag, lse, tag}, rw, lane * 16, WS_S + (g * 32 + j) * 128, AUX_SC1);
    __builtin_amdgcn_s_sleep(LSLEEP);
    u32x4 pl[4];
    gather4<8>(pl, ws, WS_S + g * 4096 + lane * 16, tag);
    float G = fmaxf(fmaxf(__uint_as_float(pl[0][0]), __uint_as_float(pl[1][0])), fmaxf(__uint_as_float(pl[2][0]), __uint_as_float(pl[3][0])));
    G = xrow_max(fmaxf(G, dpp<ROR8>(G)));
    float L = 0.f;
#pragma unroll
    for (int i = 0; i < 4; ++i) L += __builtin_amdgcn_exp2f(__uint_as_float(pl[i][0]) - G);
    L = xrow_sum(L + dpp<ROR8>(L));
    if (lane < 4) sm_lse[lane] = G + __builtin_amdgcn_logf(L);
    STAMP(6);
  }
  STAMP(4);
  if (j >= 18) {
    asm volatile("s_waitcnt lgkmcnt(0)" ::: "memory");
    STAMP_OUT(); VMDONE(); return;
  }
  if (j < 16) {
    u32x4 w2r[4], wor[8];
    const rsrc_t r2 = mkrsrc(w2, j < 14 ? DFF * DM * 2 : 0), ro = mkrsrc(wo, 4096 * DM * 2);
    BARRIER();
    STAMP(5);
#pragma unroll
    for (int i = 0; i < 4; ++i) w2r[i] = __builtin_amdgcn_raw_buffer_load_b128(r2, lg * 4096 + li * 16, (ub * 16 + i) * 1024 + hw * 256, AUX_NT);
#pragma unroll
    for (int i = 0; i < 8; ++i)
      wor[i] = __builtin_amdgcn_raw_buffer_load_b128(ro, lg * 2048 + li * 16, (g * 512 + j * 32 + 8 * (i & 3) + (i >> 2)) * 1024 + hw * 256, AUX_NT);
    const int fc = 4 * hw + lg;
    unsigned a_g = blk(WS_E2, fc * 4 + (g >> 1), (sm_w[2] >> (fc & 7)) & 1u) + ((g & 1) * 16 + j) * 128 + li * 8;
    unsigned a_l = (unsigned)(uintptr_t)(LAS float*)(sm_o + 4 * lg);
    PIN(a_g); PIN(a_l);
    BARRIER();
    float y[8] = {0, 0, 0, 0, 0, 0, 0, 0};
#pragma unroll
    for (int k = 0; k < 2; ++k)
#pragma unroll
      for (int c = 0; c < 8; ++c) y[c] = dot2(colpair(w2r[2 * k], w2r[2 * k + 1], c), hk[k], y[c]);
    unsigned wp[4][8];
#pragma unroll
    for (int k = 0; k < 4; ++k)
#pragma unroll
      for (int c = 0; c < 8; ++c) { wp[k][c] = colpair(wor[2 * k], wor[2 * k + 1], c); PIN(wp[k][c]); }
#pragma unroll
    for (int c = 0; c < 8; ++c) PIN(y[c]);
    STAMP(7);
    BARRIER();
    const u32x4 op = *reinterpret_cast<const LAS u32x4*>(a_l);
#pragma unroll
    for (int k = 0; k < 4; ++k)
#pragma unroll
      for (int c = 0; c < 8; ++c) y[c] = dot2(wp[k][c], op[k], y[c]);
    float c2[2];
    fold8(y, c2);
    __builtin_amdgcn_raw_buffer_store_b64(pack2(c2[0], c2[1], tag), rw, a_g, 0, AUX_SC1);
    STAMP(8);
  } else {
    const int fcol = 128 * (f >> 2) + 8 * li + 2 * (f & 3);
    const unsigned xo = __builtin_amdgcn_raw_buffer_load_b32(rx, fcol * 2, 0, 0);
    float x0 = bflo(xo), x1 = bfhi(xo);
    BARRIER();
    unsigned a_g = blk(WS_E2, f * 4 + hw, (sm_w[2] >> g) & 1u) + lane * 16, a_o = lane < 16 ? fcol * 2 : DM * 2;
    unsigned a_w = (unsigned)(uintptr_t)(LAS float*)(sm_o + hw * 32 + 4 * (lane & 7) + 2 * (lane >> 5)), a_r = (unsigned)(uintptr_t)(LAS float*)(sm_o + 2 * li);
    PIN(a_g); PIN(a_o); PIN(a_w); PIN(a_r); PIN(x0); PIN(x1);
    const rsrc_t rout = mkrsrc(out, hw == 0 ? DM * 2 : 0), rep = mkrsrc(ws, (hw == 1 && f == 0) ? 4 : 0);
    BARRIER();
    __builtin_amdgcn_s_sleep(E2DELAY);
    u32x4 pa[4];
    gather4<0>(pa, ws, a_g, tag);
    *reinterpret_cast<LAS f32x2*>(a_w) = sum_pieces(pa);
    BARRIER();
    STAMP(9);
    f32x2 r[4];
#pragma unroll
    for (int w = 0; w < 4; ++w) r[w] = *reinterpret_cast<const LAS f32x2*>(a_r + w * 128);
    const f32x2 t = (r[0] + r[1]) + (r[2] + r[3]);
    __builtin_amdgcn_raw_buffer_store_b32(cvtpk(x0 + t[0], x1 + t[1]), rout, a_o, 0, 0);
    __builtin_amdgcn_raw_buffer_store_b32(tag, rep, lane * 4, WS_EPOCH, AUX_SC1);
    STAMP(10);
  }
  STAMP_OUT();
  VMDONE();
}

extern "C" void kernel_launch(void* const* d_in, const int* in_sizes, int n_in, void* d_out, int out_size, void* d_ws, size_t ws_size,
                              hipStream_t stream) {
  fused_decode<<<256, 512, 0, stream>>>(d_in[1], d_in[2], d_ws, d_in[0], d_in[3], d_in[6], d_in[4], d_in[7], d_out);
}
```

```cpp
#include <hip/hip_runtime.h>
#include <hip/hip_bf16.h>
#include <stdint.h>
#include <type_traits>

constexpr int WIN = 5;
constexpr int QPOS = 26;
constexpr int CP = 216;
constexpr int CT0 = 460;
constexpr int COFF = 8;
constexpr int CG = 10;
constexpr int CW0 = 11;
constexpr int CMIN = 8;
constexpr int CMAX = 18;
constexpr int QSLEEP = 30;
constexpr int VSLEEP = 90;
constexpr int LSLEEP = 100;
constexpr int E1DELAY = 16;
constexpr int E2DELAY = 40;
constexpr int CODE_BYTES = 29900;

typedef unsigned long long u64;
typedef unsigned u32x4 __attribute__((ext_vector_type(4)));
typedef unsigned u32x2 __attribute__((ext_vector_type(2)));
typedef float f32x4 __attribute__((ext_vector_type(4)));
typedef float f32x2 __attribute__((ext_vector_type(2)));
typedef __bf16 bf16x8 __attribute__((ext_vector_type(8)));
typedef __bf16 bf16x2 __attribute__((ext_vector_type(2)));
typedef u64 u64x8 __attribute__((ext_vector_type(8)));
typedef __amdgpu_buffer_rsrc_t rsrc_t;
#define LAS __attribute__((address_space(3)))

constexpr int DM = 512, SK = 65536, DH = 128, DFF = 1792;
constexpr int NT = 16;
constexpr unsigned HEADB = SK * DH * 2;
constexpr unsigned WS_EPOCH = 0;
constexpr unsigned WS_V = 4096;
constexpr unsigned WS_Q = 16384;
constexpr unsigned WS_H = WS_Q + 8 * 16384;
constexpr unsigned WS_S = WS_H + 65536;
constexpr unsigned WS_P = WS_S + 8 * 4096;
constexpr unsigned WS_E1 = WS_P + 8 * 16384;
constexpr unsigned WS_E2 = WS_E1 + 64 * 16384;
constexpr unsigned WS_END = WS_E2 + 32 * 16384;
static_assert(WS_P % 16384 == 0 && WS_END <= (2u << 20), "layout");
static_assert(CMIN >= 2 && CMAX <= 31, "range of the wait table");
__device__ __forceinline__ unsigned blk(unsigned base, unsigned i, unsigned half) { return base + (i >> 1) * 16384 + (i & 1) * 4096 + half * 8192; }
constexpr int ACCL = 32, ACCH = 4 * ACCL + 8;
constexpr int SPIN_CAP = 1 << 18;
constexpr unsigned TAG_STEP = 0x00010001u;
constexpr int AUX_NT = 2, AUX_SC1 = 16;
constexpr float L2E = 1.4426950408889634f, SCALE = 0.088388347648318440f;

#define LAY "tjjwwnjnjntjnt"
constexpr unsigned lay_vec(int i) {
  constexpr int cb[7] = {8, 9, 10, 11, 14, 12, 13};
  return LAY[14] == '_' ? 1u << (10 + i) : i < 5 ? 1u << cb[i + 2] : ((1u << (10 + i)) | (1u << cb[(i - 5) % 7])) & ~1023u;
}
template <char C> __device__ __forceinline__ unsigned deposit(unsigned v) {
  unsigned r = 0; int k = 0;
#pragma unroll
  for (int i = 0; i < 14; ++i) if (LAY[i] == C) { r ^= ((v >> k) & 1u) * lay_vec(i); ++k; }
  return r;
}
__device__ __forceinline__ float bflo(unsigned v) { return __uint_as_float(v << 16); }
__device__ __forceinline__ float bfhi(unsigned v) { return __uint_as_float(v & 0xffff0000u); }
__device__ __forceinline__ unsigned cvtpk(float lo, float hi) {
  bf16x2 r; r[0] = (__bf16)lo; r[1] = (__bf16)hi; return __builtin_bit_cast(unsigned, r);
}
__device__ __forceinline__ float rbf(float f) { return bflo(cvtpk(f, f)); }
template <int DC> __device__ __forceinline__ float dpp(float x) {
  return __builtin_bit_cast(float, __builtin_amdgcn_mov_dpp(__builtin_bit_cast(int, x), DC, 0xf, 0xf, true));
}
constexpr int ROR1 = 0x121, ROR2 = 0x122, ROR4 = 0x124, ROR8 = 0x128, SHL4 = 0x104, SHL8 = 0x108, SHL12 = 0x10c;
__device__ __forceinline__ float xrow_max(float x) {
  auto s = __builtin_amdgcn_permlane16_swap(__float_as_uint(x), __float_as_uint(x), false, false);
  x = fmaxf(__uint_as_float(s[0]), __uint_as_float(s[1]));
  auto t = __builtin_amdgcn_permlane32_swap(__float_as_uint(x), __float_as_uint(x), false, false);
  return fmaxf(__uint_as_float(t[0]), __uint_as_float(t[1]));
}
__device__ __forceinline__ float xrow_sum(float x) {
  auto s = __builtin_amdgcn_permlane16_swap(__float_as_uint(x), __float_as_uint(x), false, false);
  x = __uint_as_float(s[0]) + __uint_as_float(s[1]);
  auto t = __builtin_amdgcn_permlane32_swap(__float_as_uint(x), __float_as_uint(x), false, false);
  return __uint_as_float(t[0]) + __uint_as_float(t[1]);
}
__device__ __forceinline__ float row_sum(float x) {
  x += dpp<ROR8>(x); x += dpp<ROR4>(x); x += dpp<ROR2>(x); x += dpp<ROR1>(x); return x;
}
__device__ __forceinline__ float wave_sum(float x) { return xrow_sum(row_sum(x)); }
__device__ __forceinline__ float dot2(unsigned a, unsigned b, float c) {
  return __builtin_amdgcn_fdot2_f32_bf16(__builtin_bit_cast(bf16x2, a), __builtin_bit_cast(bf16x2, b), c, false);
}
__device__ __forceinline__ unsigned perm_lo(unsigned hi, unsigned lo) { return __builtin_amdgcn_perm(hi, lo, 0x05040100u); }
__device__ __forceinline__ unsigned perm_hi(unsigned hi, unsigned lo) { return __builtin_amdgcn_perm(hi, lo, 0x07060302u); }
__device__ __forceinline__ unsigned colpair(const u32x4& r0, const u32x4& r1, int c) {
  return (c & 1) ? perm_hi(r1[c >> 1], r0[c >> 1]) : perm_lo(r1[c >> 1], r0[c >> 1]);
}
__device__ __forceinline__ rsrc_t mkrsrc(const void* p, unsigned bytes) { return __builtin_amdgcn_make_buffer_rsrc(const_cast<void*>(p), 0, bytes, 0x00020000); }
__device__ __forceinline__ f32x4 mfma(u32x4 a, u32x4 b, f32x4 c) {
  return __builtin_amdgcn_mfma_f32_16x16x32_bf16(__builtin_bit_cast(bf16x8, a), __builtin_bit_cast(bf16x8, b), c, 0, 0, 0);
}
__device__ __forceinline__ void fold8(const float (&a)[8], float (&c2)[2]) {
  float b4[4];
#pragma unroll
  for (int k = 0; k < 4; ++k) { auto r = __builtin_amdgcn_permlane32_swap(__float_as_uint(a[k]), __float_as_uint(a[k + 4]), false, false); b4[k] = __uint_as_float(r[0]) + __uint_as_float(r[1]); }
#pragma unroll
  for (int k = 0; k < 2; ++k) { auto r = __builtin_amdgcn_permlane16_swap(__float_as_uint(b4[k]), __float_as_uint(b4[k + 2]), false, false); c2[k] = __uint_as_float(r[0]) + __uint_as_float(r[1]); }
}

__device__ __forceinline__ u32x2 pack2(float a, float b, unsigned tag) {
  return u32x2{__builtin_amdgcn_perm(__float_as_uint(a), __float_as_uint(b), 0x07060503u), __builtin_amdgcn_perm(__float_as_uint(b), tag, 0x06050100u)};
}
__device__ __forceinline__ float unpackA(unsigned w0) { return __uint_as_float(w0); }
__device__ __forceinline__ float unpackB(unsigned w0, unsigned w1) { return __uint_as_float(__builtin_amdgcn_perm(w0, w1, 0x0403020cu)); }
__device__ __forceinline__ f32x2 sum_pieces(const u32x4 (&p)[4]) {
  float v[4];
  v[0] = (unpackA(p[0][0]) + unpackA(p[1][0])) + (unpackA(p[2][0]) + unpackA(p[3][0]));
  v[1] = (unpackB(p[0][0], p[0][1]) + unpackB(p[1][0], p[1][1])) + (unpackB(p[2][0], p[2][1]) + unpackB(p[3][0], p[3][1]));
  v[2] = (unpackA(p[0][2]) + unpackA(p[1][2])) + (unpackA(p[2][2]) + unpackA(p[3][2]));
  v[3] = (unpackB(p[0][2], p[0][3]) + unpackB(p[1][2], p[1][3])) + (unpackB(p[2][2], p[2][3]) + unpackB(p[3][2], p[3][3]));
  f32x2 r;
#pragma unroll
  for (int k = 0; k < 2; ++k) {
    auto t = __builtin_amdgcn_permlane32_swap(__float_as_uint(v[k]), __float_as_uint(v[k + 2]), false, false);
    float x = __uint_as_float(t[0]) + __uint_as_float(t[1]);
    x += dpp<ROR8>(x);
    auto u = __builtin_amdgcn_permlane16_swap(__float_as_uint(x), __float_as_uint(x), false, false);
    r[k] = __uint_as_float(u[0]) + __uint_as_float(u[1]);
  }
  return r;
}

template <int SLP>
__device__ __forceinline__ void gather4(u32x4 (&a)[4], const char* base, unsigned off, unsigned tag) {
  u64 m0, m1, m2, m3, t0; int cnt = SPIN_CAP;
  asm volatile(
      "s_mov_b64 %[m0], -1\n\ts_mov_b64 %[m1], -1\n\ts_mov_b64 %[m2], -1\n\ts_mov_b64 %[m3], -1\n\ts_nop 0\n"
      "0:\n\t"
      "s_wqm_b64 exec, %[m0]\n\tglobal_load_dwordx4 v[40:43], %[off], %[base] sc1\n\t"
      "s_wqm_b64 exec, %[m1]\n\tglobal_load_dwordx4 v[44:47], %[off], %[base] offset:1024 sc1\n\t"
      "s_wqm_b64 exec, %[m2]\n\tglobal_load_dwordx4 v[48:51], %[off], %[base] offset:2048 sc1\n\t"
      "s_wqm_b64 exec, %[m3]\n\tglobal_load_dwordx4 v[52:55], %[off], %[base] offset:3072 sc1\n\t"
      "s_mov_b64 exec, -1\n\t"
      "s_sub_u32 %[cnt], %[cnt], 1\n\ts_cbranch_scc1 9f\n\t"
      "s_waitcnt vmcnt(0)\n\t"
      "v_perm_b32 v56, v43, v41, %[sel]\n\tv_perm_b32 v57, v47, v45, %[sel]\n\tv_perm_b32 v58, v51, v49, %[sel]\n\tv_perm_b32 v59, v55, v53, %[sel]\n\t"
      "v_cmp_ne_u32 %[m0], %[tag], v56\n\tv_cmp_ne_u32 %[m1], %[tag], v57\n\tv_cmp_ne_u32 %[m2], %[tag], v58\n\tv_cmp_ne_u32 %[m3], %[tag], v59\n\t"
      ".if %c[slp]\n\ts_sleep %c[slp]\n\t.endif\n\t"
      "s_or_b64 %[t0], %[m0], %[m1]\n\ts_or_b64 vcc, %[m2], %[m3]\n\ts_or_b64 %[t0], %[t0], vcc\n\t"
      "s_cbranch_scc1 0b\n"
      "9:\n\ts_waitcnt vmcnt(0)"
      : "=&{v[40:43]}"(a[0]), "=&{v[44:47]}"(a[1]), "=&{v[48:51]}"(a[2]), "=&{v[52:55]}"(a[3]),
        [m0] "=&s"(m0), [m1] "=&s"(m1), [m2] "=&s"(m2), [m3] "=&s"(m3), [t0] "=&s"(t0), [cnt] "+s"(cnt)
      : [tag] "s"(tag), [base] "s"(base), [off] "v"(off), [sel] "s"(0x05040100u), [slp] "i"(SLP)
      : "memory", "vcc", "scc", "v56", "v57", "v58", "v59");
}
__device__ __forceinline__ unsigned rot16(unsigned t) { return (t >> 16) | (t << 16); }
__device__ __forceinline__ u32x4 piece(unsigned a, unsigned b, unsigned tag) { return u32x4{a, tag, b, rot16(tag)}; }
__device__ __forceinline__ u32x4 load_dev(const char* base, unsigned off) {
  u32x4 a;
  asm volatile("global_load_dwordx4 %0, %1, %2 sc1\n\ts_waitcnt vmcnt(0)" : "=&v"(a) : "v"(off), "s"(base) : "memory");
  return a;
}
__device__ __forceinline__ u32x4 gather1(const char* base, unsigned off, unsigned tag) {
  u32x4 p;
#pragma unroll 1
  for (int spin = 0; spin < SPIN_CAP; ++spin) {
    p = load_dev(base, off);
    if (__builtin_amdgcn_ballot_w64(((p[1] ^ tag) | (p[3] ^ rot16(tag))) != 0) == 0) break;
    __builtin_amdgcn_s_sleep(32);
  }
  return p;
}
template <int SLP>
__device__ __forceinline__ void lds_wait(const unsigned* flags, int idx) {
#pragma unroll 1
  for (int spin = 0; spin < 32 * SPIN_CAP; ++spin) {
    if (__builtin_amdgcn_ballot_w64(*(volatile LAS unsigned*)(flags + idx) == 0u) == 0) break;
    __builtin_amdgcn_s_sleep(SLP);
  }
  asm volatile("" ::: "memory");
}
__device__ __forceinline__ void lds_set(unsigned* flag) {
  asm volatile("" ::: "memory");
  *(volatile LAS unsigned*)flag = 1u;
}
__device__ __forceinline__ int timed_read(const char* p) {
  u64 t0, t1; unsigned d;
  asm volatile("s_memtime %0\n\ts_waitcnt lgkmcnt(0)\n\ts_load_dword %2, %3, 0\n\ts_waitcnt lgkmcnt(0)\n\ts_memtime %1\n\ts_waitcnt lgkmcnt(0)"
               : "=&s"(t0), "=&s"(t1), "=&s"(d) : "s"(p) : "memory");
  return (int)((unsigned)t1 - (unsigned)t0);
}

#define SB() __builtin_amdgcn_sched_barrier(0)
#define SAME_PAGE(n) do { SB(); asm volatile("s_branch 1f\n\t.p2align 12,," #n "\n1:"); SB(); } while (0)
#define PIN(x) asm volatile("" : "+v"(x))
#define PACE(w) do { SB(); asm volatile("s_waitcnt vmcnt(%0)" :: "i"((w) - 1) : "memory"); } while (0)
#define PACEV(t) do { SB(); asm volatile("s_swappc_b64 s[96:97], %0" :: "s"(tgt[(t) & 1]) : "memory", "s96", "s97"); } while (0)
#define VMDONE() __builtin_amdgcn_s_waitcnt(0x0f70)
#define BARRIER() do { SB(); asm volatile("s_waitcnt lgkmcnt(0)\n\ts_barrier" ::: "memory"); SB(); } while (0)

__global__ void __launch_bounds__(512, 2) __attribute__((aligned(4096)))
fused_decode(const void* K_, const void* V_, void* ws_, const void* x_, const void* wq_, const void* w1_, const void* wo_, const void* w2_, void* out_) {
  __shared__ __attribute__((aligned(16))) float lds[2048 + 16 * ACCH + 128 + 64 + 4 * NT * 256];
  float* const sm_q = lds;
  float* const sm_acc = lds + 2048;
  float* const sm_o = sm_acc + 16 * ACCH;
  float* const sm_m = sm_o + 128;
  float* const sm_lse = sm_m + 16;
  unsigned* const sm_w = reinterpret_cast<unsigned*>(sm_lse + 8);
  unsigned* const sm_flag = sm_w + 8;
  float* const sm_s = sm_m + 64;
  asm volatile("s_load_dwordx16 s[64:79], %0, 0\n\ts_load_dwordx2 s[80:81], %0, 0x40" :: "s"(__builtin_amdgcn_kernarg_segment_ptr())
               : "s64", "s65", "s66", "s67", "s68", "s69", "s70", "s71", "s72", "s73", "s74", "s75", "s76", "s77", "s78", "s79", "s80", "s81");
  SB();
  const int b = blockIdx.x, g = b & 7, j = b >> 3;
  const int tid = threadIdx.x, lane = tid & 63, wave = __builtin_amdgcn_readfirstlane(tid >> 6);
  const int lg = lane >> 4, li = lane & 15;
  unsigned koff = (li >> 2) * 256 + lg * 64 + (li & 3) * 16, voff = lane * 16;
  const unsigned wbase = deposit<'j'>(j) ^ deposit<'w'>(wave);
  unsigned qoff = (lane >> 3) * 16 * 8192 + (lane & 7) * 16, xoff = (lane >> 3) * 32;
  if (tid < 24) sm_flag[tid] = 0u;
  u64x8 ka; u64 ka8;
  {
    u64 pc;
    asm volatile("s_cmp_lg_u32 %8, 7\n\ts_cbranch_scc1 1f\n\ts_getpc_b64 %2\n\ts_lshr_b64 %2, %2, 12\n\ts_lshl_b64 %2, %2, 12\n\tglobal_load_dword v255, %7, %2\n1:\n\t"
                 "s_waitcnt lgkmcnt(0)"
                 : "=&{s[64:79]}"(ka), "=&{s[80:81]}"(ka8), "=&s"(pc), "+v"(koff), "+v"(voff), "+v"(qoff), "+v"(xoff)
                 : "v"(min(j * 1024 + (lane & 7) * 128, CODE_BYTES - 4)), "s"(wave) : "memory", "scc", "v255");
  }
  const char *K = (const char*)ka[0], *V = (const char*)ka[1], *x = (const char*)ka[3], *wq = (const char*)ka[4], *w1 = (const char*)ka[5], *wo = (const char*)ka[6], *w2 = (const char*)ka[7];
  char *ws = (char*)((ka[2] + 0x1fffffull) & ~0x1fffffull), *out = (char*)ka8;
  const int hw = wave - 4;
  const rsrc_t rx = mkrsrc(x, DM * 2);
  const rsrc_t rw = mkrsrc(ws, WS_END);
  unsigned tag = 0;

  u32x4 xr, xa, xb, wr[16];
  auto proj_load = [&](const char* W, unsigned ldb, unsigned woff, int rb, unsigned colb) {
    xr = __builtin_amdgcn_raw_buffer_load_b128(rx, voff, 0, 0);
    xa = __builtin_amdgcn_raw_buffer_load_b128(rx, xoff, rb * 256, 0); xb = __builtin_amdgcn_raw_buffer_load_b128(rx, xoff + 16, rb * 256, 0);
    const rsrc_t rm = mkrsrc(W, DM * ldb);
#pragma unroll
    for (int i = 0; i < 16; ++i) wr[i] = __builtin_amdgcn_raw_buffer_load_b128(rm, woff, (128 * rb + i) * ldb + colb, AUX_NT);
    SB();
  };
  auto proj_send = [&](unsigned dst) {
    float xf[8];
#pragma unroll
    for (int k = 0; k < 4; ++k) { xf[2 * k] = bflo(xr[k]); xf[2 * k + 1] = bfhi(xr[k]); }
    const float mean = wave_sum(((xf[0] + xf[1]) + (xf[2] + xf[3])) + ((xf[4] + xf[5]) + (xf[6] + xf[7]))) * (1.f / 512.f);
    float vs = 0.f;
#pragma unroll
    for (int k = 0; k < 8; ++k) { const float d = xf[k] - mean; vs += d * d; }
    const float var = wave_sum(vs) * (1.f / 512.f);
    const float mb = rbf(mean), rden = __builtin_amdgcn_rcpf(rbf(__builtin_amdgcn_sqrtf(rbf(rbf(var) + 1e-5f))));
    auto ln2 = [&](unsigned xp) -> unsigned { return cvtpk(rbf(bflo(xp) - mb) * rden, rbf(bfhi(xp) - mb) * rden); };
    unsigned xn[8];
#pragma unroll
    for (int k = 0; k < 4; ++k) { xn[k] = ln2(xa[k]); xn[4 + k] = ln2(xb[k]); }
    float a[8] = {0, 0, 0, 0, 0, 0, 0, 0};
#pragma unroll
    for (int k = 0; k < 8; ++k)
#pragma unroll
      for (int c = 0; c < 8; ++c) a[c] = dot2(colpair(wr[2 * k], wr[2 * k + 1], c), xn[k], a[c]);
    float c2[2];
    fold8(a, c2);
    c2[0] += dpp<ROR8>(c2[0]); c2[1] += dpp<ROR8>(c2[1]);
    if (li < 8) __builtin_amdgcn_raw_buffer_store_b128(piece(__float_as_uint(c2[0]), __float_as_uint(c2[1]), tag), rw, lg * 128 + li * 16, dst, AUX_SC1);
  };
  unsigned epoch;
  if (wave == 4) {
    epoch = __builtin_amdgcn_raw_buffer_load_b32(rw, 0, WS_EPOCH, AUX_SC1);
    proj_load(wq, 8192u, qoff, j >> 3, g * 1024 + (j & 7) * 128);
  }
  SB();
  asm volatile("s_barrier" ::: "memory");
  SB();

  const rsrc_t rk = mkrsrc(K + (size_t)g * HEADB, HEADB), rv = mkrsrc(V + (size_t)g * HEADB, HEADB);
  auto toff = [&](int n) -> unsigned { return wbase ^ deposit<'n'>(n); };
  auto ldk = [&](unsigned so, int t) -> u32x4 { return __builtin_amdgcn_raw_buffer_load_b128(rk, koff, so ^ deposit<'t'>(t), AUX_NT); };
  auto ldv = [&](unsigned so, int t) -> u32x4 { return __builtin_amdgcn_raw_buffer_load_b128(rv, voff, so ^ deposit<'t'>(t), AUX_NT); };
  u32x4 ring[4][8];
  int role = wave >> 2;
  asm volatile("" : "+s"(role));
  if (role == 0) {
#define KFIRST(i0, i1) _Pragma("unroll") for (int i = i0; i < i1; ++i) { \
      if (i >= WIN) PACE(WIN); \
      ring[i >> 3][i & 7] = ldk(toff(i >> 3), i & 7); SB(); }
    KFIRST(0, QPOS)
    lds_wait<1>(sm_flag, lane & 3);
    KFIRST(QPOS, QPOS + 1)
    f32x4 qa[4], qc[4];
    {
      const float* qp = sm_q + (li & 3) * 128 + 8 * (4 * lg + (li >> 2));
#pragma unroll
      for (int r = 0; r < 4; ++r) { qa[r] = *reinterpret_cast<const f32x4*>(qp + 512 * r); qc[r] = *reinterpret_cast<const f32x4*>(qp + 512 * r + 4); }
    }
    const unsigned tagv = sm_w[0];
    KFIRST(QPOS + 1, QPOS + 2)
    u32x4 qb;
    {
      const f32x4 a = ((qa[0] + qa[1]) + qa[2]) + qa[3], c = ((qc[0] + qc[1]) + qc[2]) + qc[3];
      qb = u32x4{cvtpk(a[0], a[1]), cvtpk(a[2], a[3]), cvtpk(c[0], c[1]), cvtpk(c[2], c[3])};
      PIN(qb);
    }
    tag = __builtin_amdgcn_readfirstlane(tagv);
    KFIRST(QPOS + 2, 32)
#undef KFIRST
    u64 tbl, tgt[2];
    asm volatile("s_getpc_b64 %0\n\ts_branch 1f\n\t.set w, 0\n\t.rept 16\n\ts_waitcnt vmcnt(w)\n\ts_setpc_b64 s[96:97]\n\t.set w, w+1\n\t.endr\n1:" : "=s"(tbl));
    auto steer = [&](int n) {
      const unsigned now = (unsigned)__builtin_amdgcn_s_memrealtime();
      const unsigned lag = now - __builtin_amdgcn_readfirstlane(*(volatile LAS unsigned*)(sm_w + 1)) - (unsigned)((n * CP) >> 1);
      const int w2 = min(max(CW0 + ((int)(lag * CG) >> 8), CMIN), CMAX);
      tgt[0] = tbl + (unsigned)(8 * (w2 >> 1) - 4); tgt[1] = tbl + (unsigned)(8 * ((w2 + 1) >> 1) - 4);
    };
    *(volatile LAS unsigned*)(sm_flag + 12 + wave) = (unsigned)__builtin_amdgcn_s_memrealtime() | 1u;
    u32x4* const sp = reinterpret_cast<u32x4*>(sm_s) + wave * (NT * 64) + lane;
    float mraw = -3.0e38f;
#pragma unroll 1
    for (int it = 0; it < NT; it += 4) {
      const bool last = it == NT - 4;
      const rsrc_t rn = mkrsrc(last ? V + (size_t)g * HEADB : K + (size_t)g * HEADB, HEADB);
      const unsigned noff = last ? voff : koff;
#pragma unroll
      for (int u = 0; u < 4; ++u) {
        steer(it + u);
        const unsigned so = toff((it + u + 4) & (NT - 1));
        u32x4 sc;
        float v[2];
#pragma unroll
        for (int t = 0; t < 8; ++t) {
          const f32x4 d = mfma(ring[u][t], qb, f32x4{0, 0, 0, 0});
          PACEV(t);
          ring[u][t] = __builtin_amdgcn_raw_buffer_load_b128(rn, noff, so ^ deposit<'t'>(t), AUX_NT);
          SB();
          v[t & 1] = ((d[0] + dpp<SHL4>(d[1])) + dpp<SHL8>(d[2])) + dpp<SHL12>(d[3]);
          if (t & 1) {
            const unsigned r = cvtpk(v[0], v[1]);
            sc[t >> 1] = cvtpk(bflo(r) * SCALE, bfhi(r) * SCALE);
            mraw = fmaxf(mraw, fmaxf(v[0], v[1]));
          }
        }
        sp[(it + u) * 64] = sc;
      }
    }
    mraw = rbf(rbf(xrow_max(mraw)) * SCALE);
    if (lane < 4) sm_m[wave * 4 + lane] = mraw;
    lds_set(sm_flag + 4 + wave);
    float mref = *(volatile LAS float*)(sm_m + wave * 4 + (li & 3)), lg2 = 0.f;
    const u32x4* const sr = sp - li + (li & 3);
    f32x4 acc[2] = {{0, 0, 0, 0}, {0, 0, 0, 0}};
    auto probs = [&](unsigned sc) -> unsigned {
      return cvtpk(__builtin_amdgcn_exp2f(__builtin_fmaf(bflo(sc) - mref, L2E, -lg2)), __builtin_amdgcn_exp2f(__builtin_fmaf(bfhi(sc) - mref, L2E, -lg2)));
    };
    auto only = [&](const u32x4& p, int sx) -> u32x4 { return (li >> 2) == sx ? p : u32x4{0u, 0u, 0u, 0u}; };
    auto pv = [&](const u32x4 (&vc)[8], int n, u32x4 (&vn)[8], int nn, auto t0, auto ns) {
      constexpr int T0 = decltype(t0)::value, NS = decltype(ns)::value;
      const unsigned so = toff(nn);
#define VSTEP(i) do { if constexpr (NS && ((i) * NS) % 8 == 0) { constexpr int t = T0 + (i) * NS / 8; PACEV(t); vn[t] = ldv(so, t); SB(); } } while (0)
      const u32x4 sc = sr[n * 64];
      VSTEP(0);
      u32x4 pb;
      pb[0] = probs(sc[0]); pb[1] = probs(sc[1]);
      VSTEP(1);
      pb[2] = probs(sc[2]); pb[3] = probs(sc[3]);
      VSTEP(2);
      VSTEP(3);
      auto two = [&](int sx) {
        const u32x4 ps = only(pb, sx);
#pragma unroll
        for (int a = 0; a < 2; ++a) {
          u32x4 f;
#pragma unroll
          for (int k = 0; k < 4; ++k) f[k] = colpair(vc[2 * k], vc[2 * k + 1], 4 * a + sx);
          acc[a] = mfma(f, ps, acc[a]);
        }
      };
      two(0); VSTEP(4); two(1); VSTEP(5); two(2); VSTEP(6); two(3); VSTEP(7);
#undef VSTEP
    };
    using I0 = std::integral_constant<int, 0>;
    using I4 = std::integral_constant<int, 4>;
    using I8 = std::integral_constant<int, 8>;
    pv(ring[0], 0, ring[3], 0, I0{}, I0{});
#pragma unroll 1
    for (int it = 1; it < NT - 7; it += 4) {
      steer(15 + it);
      pv(ring[1], it, ring[0], it + 3, I0{}, I8{});
      steer(16 + it);
      pv(ring[2], it + 1, ring[1], it + 4, I0{}, I8{});
      steer(17 + it);
      pv(ring[3], it + 2, ring[2], it + 5, I0{}, I8{});
      steer(18 + it);
      pv(ring[0], it + 3, ring[3], it + 6, I0{}, I8{});
    }
    steer(24);
    pv(ring[1], 9, ring[0], 12, I0{}, I8{});
    steer(25);
    pv(ring[2], 10, ring[1], 13, I0{}, I8{});
    steer(26);
    lds_wait<1>(sm_flag, 10 + (lane & 1));
    {
      const f32x2 gl = *reinterpret_cast<const f32x2*>(sm_lse + 2 * (li & 3));
      const float fs = __builtin_amdgcn_exp2f(__builtin_fmaf(mref - gl[0], L2E, -gl[1]));
      mref = gl[0]; lg2 = gl[1];
      acc[0] *= fs; acc[1] *= fs;
    }
    pv(ring[3], 11, ring[2], 14, I0{}, I8{});
    const unsigned half = (sm_w[2] >> g) & 1u;
    unsigned a_wr = (unsigned)(uintptr_t)(LAS float*)(sm_acc + (wave * 4 + (li & 3)) * ACCH + lg * ACCL + 4 * (li >> 2));
    unsigned a_rd = (unsigned)(uintptr_t)(LAS float*)(sm_acc + wave * ACCH + 2 * lane);
    unsigned a_o = (unsigned)(uintptr_t)(LAS float*)(sm_o + 2 * (lane & 7) + (lane >> 5));
    unsigned a_g = blk(WS_E1, g * 16 + wave * 4 + lg, half) + j * 128 + li * 8, a_in = blk(WS_E1, g * 16 + (j & 15), half) + lane * 16;
    unsigned tagl = tag >> (16 * (li & 1));
    PIN(a_wr); PIN(a_rd); PIN(a_o); PIN(a_g); PIN(a_in); PIN(tagl);
    steer(27);
    pv(ring[0], 12, ring[3], 15, I0{}, I4{});
    SAME_PAGE(2900);
    pv(ring[1], 13, ring[3], 15, I4{}, I4{});
    lds_set(sm_flag + 16 + wave);
    pv(ring[2], 14, ring[3], 0, I0{}, I0{});
    {
      const u32x4 sc = sr[(NT - 1) * 64];
      u32x4 pb, ps[4], f[8];
#pragma unroll
      for (int k = 0; k < 4; ++k) pb[k] = probs(sc[k]);
#pragma unroll
      for (int sx = 0; sx < 4; ++sx) ps[sx] = only(pb, sx);
#pragma unroll
      for (int k = 0; k < 3; ++k) {
        SB();
#pragma unroll
        for (int e = 0; e < 8; ++e) f[e][k] = colpair(ring[3][2 * k], ring[3][2 * k + 1], e);
      }
      SB();
#pragma unroll
      for (int e = 0; e < 8; ++e) { f[e][3] = colpair(ring[3][6], ring[3][7], e); acc[e >> 2] = mfma(f[e], ps[e & 3], acc[e >> 2]); }
    }
    *reinterpret_cast<LAS f32x4*>(a_wr) = acc[0];
    *reinterpret_cast<LAS f32x4*>(a_wr + 64) = acc[1];
    BARRIER();
    {
      const f32x2 v = (*reinterpret_cast<const LAS f32x2*>(a_rd) + *reinterpret_cast<const LAS f32x2*>(a_rd + 16 * ACCH)) +
                      (*reinterpret_cast<const LAS f32x2*>(a_rd + 32 * ACCH) + *reinterpret_cast<const LAS f32x2*>(a_rd + 48 * ACCH));
      __builtin_amdgcn_raw_buffer_store_b64(pack2(v[0], v[1], tagl), rw, a_g, 0, AUX_SC1);
    }
    if (wave == 2 && j < 16) {
      __builtin_amdgcn_s_sleep(E1DELAY);
      u32x4 pa[4];
      gather4<0>(pa, ws, a_in, tag);
      const f32x2 o = sum_pieces(pa);
      *reinterpret_cast<LAS unsigned*>(a_o) = cvtpk(o[0], o[1]);
    }
    BARRIER();
    asm volatile("s_endpgm" : "=v"(wr[0]), "=v"(wr[1]), "=v"(wr[2]), "=v"(wr[3]), "=v"(wr[4]), "=v"(wr[5]), "=v"(wr[6]), "=v"(wr[7]), "=v"(wr[8]), "=v"(wr[9]),
                 "=v"(wr[10]), "=v"(wr[11]), "=v"(wr[12]), "=v"(wr[13]), "=v"(wr[14]), "=v"(wr[15]), "=v"(xr), "=v"(xa), "=v"(xb), "=v"(epoch) :: "memory");
  }

  if (wave == 4) {
    tag = __builtin_amdgcn_readfirstlane(epoch) + TAG_STEP;
    SB();
    const unsigned t0 = (unsigned)__builtin_amdgcn_s_memrealtime();
    *reinterpret_cast<u32x2*>(sm_w) = u32x2{tag, t0 + CT0};
    proj_send(WS_Q + g * 16384 + (j >> 3) * 4096 + (j & 7) * 512);
    lds_set(sm_flag + 8);
  }
  int vote = 0;
  auto sample = [&](int k) {
    const char* p = ws + WS_P + k * 16384 + (((j - 24 + 4 * k) & 7) * 8 + g) * 128;
    vote += min(max(timed_read(p) - timed_read(p + 8192), -600), 600);
  };
  if (hw == 3 && j >= 24) {
    timed_read(ws + WS_P + 7 * 16384);
    sample(0);
  }
  lds_wait<1>(sm_flag, 8);
  tag = __builtin_amdgcn_readfirstlane(sm_w[0]);
  __builtin_amdgcn_s_sleep(QSLEEP);
  {
    u32x4 pq[4];
    gather4<0>(pq, ws, WS_Q + g * 16384 + hw * 4096 + lane * 16, tag);
#pragma unroll
    for (int i = 0; i < 4; ++i) *reinterpret_cast<u32x2*>(sm_q + hw * 512 + i * 128 + (lane >> 5) * 64 + (lane & 7) * 8 + ((lane >> 3) & 3) * 2) = u32x2{pq[i][0], pq[i][2]};
    lds_set(sm_flag + hw);
  }
  if (hw == 1 && j >= 18) {
    const int item = g * 14 + j - 18;
    __builtin_amdgcn_s_sleep(64);
    proj_load(w1, DFF * 2, (lane >> 3) * 16 * (DFF * 2) + (lane & 7) * 16, item & 3, (item >> 2) * 128);
    proj_send(WS_H + (item & 3) * 14336 + (item >> 2) * 512);
  }
  const int f = (j - 16) * 8 + g;
  if (hw == 3) {
    if (j >= 24) {
#pragma unroll 1
      for (int k = 1; k < 3; ++k) sample(k);
    }
    lds_wait<4>(sm_flag, 12);
    const unsigned t1 = sm_flag[12];
    if (lane < 4 && j >= 24) __builtin_amdgcn_raw_buffer_store_b128(piece((unsigned)vote, t1, tag), rw, lane * 16, WS_V + (g * 8 + j - 24) * 128, AUX_SC1);
    __builtin_amdgcn_s_sleep(VSLEEP);
    const u32x4 vt = gather1(ws, WS_V + lane * 128, tag);
    *(volatile LAS unsigned*)(sm_w + 1) = t1 + COFF + (int)(wave_sum((float)(int)(vt[2] - t1)) * (1.f / 64.f));
    int d = (int)vt[0];
    d += __builtin_amdgcn_mov_dpp(d, 0xB1, 0xf, 0xf, true); d += __builtin_amdgcn_mov_dpp(d, 0x4E, 0xf, 0xf, true); d += __builtin_amdgcn_mov_dpp(d, 0x141, 0xf, 0xf, true);
    const unsigned halves = (unsigned)(((__builtin_amdgcn_ballot_w64(d > 0) & 0x0101010101010101ull) * 0x0102040810204080ull) >> 56);
    sm_w[2] = halves;
    lds_set(sm_flag + 11);
    const int nb = j < 16 ? 1 : j < 18 ? 4 : 0;
#pragma unroll 1
    for (int i = 0; i < nb; ++i) {
      u32x4 dd[4];
      const unsigned o = blk(j < 16 ? WS_E1 : WS_E2, j < 16 ? g * 16 + j : f * 4 + i, (halves >> g) & 1u) + lane * 16;
#pragma unroll
      for (int k = 0; k < 4; ++k) dd[k] = __builtin_amdgcn_raw_buffer_load_b128(rw, o + k * 1024, 0, AUX_SC1);
      asm volatile("" :: "v"(dd[0]), "v"(dd[1]), "v"(dd[2]), "v"(dd[3]));
    }
  }
  unsigned hk[2] = {0u, 0u};
  const int ub = g * 14 + j;
  if (j < 14) {
    for (int q = hw == 3 ? 5 : 0; q < 6; ++q) __builtin_amdgcn_s_sleep(64);
    const int u = ub * 16 + 4 * lg + 2 * (li & 1);
    const u32x4 ph = gather1(ws, WS_H + (li >> 2) * 14336 + (u >> 6) * 512 + ((u >> 1) & 3) * 128 + ((u >> 3) & 7) * 16, tag);
    float h0 = __uint_as_float(ph[0]), h1 = __uint_as_float(ph[2]);
    h0 += dpp<ROR8>(h0); h1 += dpp<ROR8>(h1); h0 += dpp<ROR4>(h0); h1 += dpp<ROR4>(h1);
    const float hp = __uint_as_float(cvtpk(fmaxf(h0, 0.f), fmaxf(h1, 0.f)));
    hk[0] = __float_as_uint(dpp<0x00>(hp)); hk[1] = __float_as_uint(dpp<0x55>(hp));
  }
  if (hw == 2) {
    lds_wait<8>(sm_flag, 4 + (lane & 3));
    const int h = lane & 3;
    const float M = fmaxf(fmaxf(sm_m[h], sm_m[4 + h]), fmaxf(sm_m[8 + h], sm_m[12 + h]));
    const u32x4* sp = reinterpret_cast<const u32x4*>(sm_s) + lg * 64 + (li >> 2) * 16 + h;
    float l = 0.f;
#pragma unroll 2
    for (int i = 0; i < NT; ++i) {
      const u32x4 sc = sp[i * 256];
#pragma unroll
      for (int k = 0; k < 4; ++k) l += __builtin_amdgcn_exp2f((bflo(sc[k]) - M) * L2E) + __builtin_amdgcn_exp2f((bfhi(sc[k]) - M) * L2E);
    }
    l += dpp<ROR8>(l); l += dpp<ROR4>(l);
    l = xrow_sum(l);
    if (lane < 8) __builtin_amdgcn_raw_buffer_store_b128(piece(__float_as_uint(M), __float_as_uint(l), tag), rw, lane * 16, WS_S + (g * 32 + j) * 128, AUX_SC1);
    __builtin_amdgcn_s_sleep(LSLEEP);
    u32x4 pl[4];
    gather4<8>(pl, ws, WS_S + g * 4096 + lane * 16, tag);
    float G = fmaxf(fmaxf(__uint_as_float(pl[0][0]), __uint_as_float(pl[1][0])), fmaxf(__uint_as_float(pl[2][0]), __uint_as_float(pl[3][0])));
    G = xrow_max(fmaxf(G, dpp<ROR8>(G)));
    float L = 0.f;
#pragma unroll
    for (int i = 0; i < 4; ++i) L += __uint_as_float(pl[i][2]) * __builtin_amdgcn_exp2f((__uint_as_float(pl[i][0]) - G) * L2E);
    L = xrow_sum(L + dpp<ROR8>(L));
    if (lane < 4) *reinterpret_cast<f32x2*>(sm_lse + 2 * lane) = f32x2{G, __builtin_amdgcn_logf(L)};
    lds_set(sm_flag + 10);
  }
  if (j >= 18) {
    asm volatile("s_waitcnt lgkmcnt(0)" ::: "memory");
    VMDONE(); return;
  }
  if (j < 16) {
    u32x4 w2r[4], wor[8];
    const rsrc_t r2 = mkrsrc(w2, j < 14 ? DFF * DM * 2 : 0), ro = mkrsrc(wo, 4096 * DM * 2);
    SAME_PAGE(1400);
    lds_wait<1>(sm_flag, 16 + (lane & 3));
#pragma unroll
    for (int i = 0; i < 4; ++i) w2r[i] = __builtin_amdgcn_raw_buffer_load_b128(r2, lg * 4096 + li * 16, (ub * 16 + i) * 1024 + hw * 256, AUX_NT);
#pragma unroll
    for (int i = 0; i < 8; ++i)
      wor[i] = __builtin_amdgcn_raw_buffer_load_b128(ro, lg * 2048 + li * 16, (g * 512 + j * 32 + 8 * (i & 3) + (i >> 2)) * 1024 + hw * 256, AUX_NT);
    const int fc = 4 * hw + lg;
    unsigned a_g = blk(WS_E2, fc * 4 + (g >> 1), (sm_w[2] >> (fc & 7)) & 1u) + ((g & 1) * 16 + j) * 128 + li * 8;
    unsigned a_l = (unsigned)(uintptr_t)(LAS float*)(sm_o + 4 * lg);
    unsigned tagl = tag >> (16 * (li & 1));
    PIN(a_g); PIN(a_l); PIN(tagl);
    BARRIER();
    float y[8] = {0, 0, 0, 0, 0, 0, 0, 0};
#pragma unroll
    for (int k = 0; k < 2; ++k)
#pragma unroll
      for (int c = 0; c < 8; ++c) y[c] = dot2(colpair(w2r[2 * k], w2r[2 * k + 1], c), hk[k], y[c]);
    unsigned wp[4][8];
#pragma unroll
    for (int k = 0; k < 4; ++k)
#pragma unroll
      for (int c = 0; c < 8; ++c) { wp[k][c] = colpair(wor[2 * k], wor[2 * k + 1], c); PIN(wp[k][c]); }
#pragma unroll
    for (int c = 0; c < 8; ++c) PIN(y[c]);
    BARRIER();
    const u32x4 op = *reinterpret_cast<const LAS u32x4*>(a_l);
#pragma unroll
    for (int k = 0; k < 4; ++k)
#pragma unroll
      for (int c = 0; c < 8; ++c) y[c] = dot2(wp[k][c], op[k], y[c]);
    float c2[2];
    fold8(y, c2);
    __builtin_amdgcn_raw_buffer_store_b64(pack2(c2[0], c2[1], tagl), rw, a_g, 0, AUX_SC1);
  } else {
    const int fcol = 128 * (f >> 2) + 8 * li + 2 * (f & 3);
    const unsigned xo = __builtin_amdgcn_raw_buffer_load_b32(rx, fcol * 2, 0, 0);
    float x0 = bflo(xo), x1 = bfhi(xo);
    lds_wait<8>(sm_flag, 11);
    SAME_PAGE(1000);
    unsigned a_g = blk(WS_E2, f * 4 + hw, (sm_w[2] >> g) & 1u) + lane * 16, a_o = lane < 16 ? fcol * 2 : DM * 2;
    unsigned a_w = (unsigned)(uintptr_t)(LAS float*)(sm_o + hw * 32 + 4 * (lane & 7) + 2 * (lane >> 5)), a_r = (unsigned)(uintptr_t)(LAS float*)(sm_o + 2 * li);
    PIN(a_g); PIN(a_o); PIN(a_w); PIN(a_r); PIN(x0); PIN(x1);
    const rsrc_t rout = mkrsrc(out, hw == 0 ? DM * 2 : 0), rep = mkrsrc(ws, (hw == 1 && f == 0) ? 4 : 0);
    BARRIER();
    __builtin_amdgcn_s_sleep(E2DELAY);
    u32x4 pa[4];
    gather4<0>(pa, ws, a_g, tag);
    *reinterpret_cast<LAS f32x2*>(a_w) = sum_pieces(pa);
    BARRIER();
    f32x2 r[4];
#pragma unroll
    for (int w = 0; w < 4; ++w) r[w] = *reinterpret_cast<const LAS f32x2*>(a_r + w * 128);
    const f32x2 t = (r[0] + r[1]) + (r[2] + r[3]);
    __builtin_amdgcn_raw_buffer_store_b32(cvtpk(x0 + t[0], x1 + t[1]), rout, a_o, 0, 0);
    __builtin_amdgcn_raw_buffer_store_b32(tag, rep, lane * 4, WS_EPOCH, AUX_SC1);
  }
  VMDONE();
}

extern "C" void kernel_launch(void* const* d_in, const int* in_sizes, int n_in, void* d_out, int out_size, void* d_ws, size_t ws_size,
                              hipStream_t stream) {
  fused_decode<<<256, 512, 0, stream>>>(d_in[1], d_in[2], d_ws, d_in[0], d_in[3], d_in[6], d_in[4], d_in[7], d_out);
}
```

```cpp
#include <hip/hip_runtime.h>
#include <hip/hip_bf16.h>
#include <stdint.h>
#include <type_traits>

constexpr int WIN = 5;
constexpr int QPOS = 26;
constexpr int CP = 214;
constexpr int CT0 = 460;
constexpr int COFF = 0;
constexpr int CG = 10;
constexpr int CW0 = 11;
constexpr int CMIN = 8;
constexpr int CMAX = 18;
constexpr int QSLEEP = 30;
constexpr int QSLEEP0 = 16;
constexpr int HSLEEP = 120;
constexpr int VSLEEP = 90;
constexpr int LSLEEP = 100;
constexpr int E1DELAY = 16;
constexpr int E2DELAY = 40;
constexpr int CODE_BYTES = 27500;

typedef unsigned long long u64;
typedef unsigned u32x4 __attribute__((ext_vector_type(4)));
typedef unsigned u32x2 __attribute__((ext_vector_type(2)));
typedef float f32x4 __attribute__((ext_vector_type(4)));
typedef float f32x2 __attribute__((ext_vector_type(2)));
typedef __bf16 bf16x8 __attribute__((ext_vector_type(8)));
typedef __bf16 bf16x2 __attribute__((ext_vector_type(2)));
typedef u64 u64x8 __attribute__((ext_vector_type(8)));
typedef __amdgpu_buffer_rsrc_t rsrc_t;
#define LAS __attribute__((address_space(3)))

constexpr int DM = 512, SK = 65536, DH = 128, DFF = 1792;
constexpr int NT = 16;
constexpr unsigned HEADB = SK * DH * 2;
constexpr unsigned WS_EPOCH = 0;
constexpr unsigned WS_V = 4096;
constexpr unsigned WS_Q = 16384;
constexpr unsigned WS_H = WS_Q + 8 * 16384;
constexpr unsigned WS_S = WS_H + 8 * 14336;
constexpr unsigned WS_P = WS_S + 8 * 4096;
constexpr unsigned WS_E1 = WS_P + 8 * 16384;
constexpr unsigned WS_E2 = WS_E1 + 64 * 16384;
constexpr unsigned WS_END = WS_E2 + 32 * 16384;
static_assert(WS_P % 16384 == 0 && WS_END <= (2u << 20), "layout");
static_assert(CMIN >= 2 && CMAX <= 31, "range of the wait table");
__device__ __forceinline__ unsigned blk(unsigned base, unsigned i, unsigned half) { return base + (i >> 1) * 16384 + (i & 1) * 4096 + half * 8192; }
constexpr int ACCL = 32, ACCH = 4 * ACCL + 8;
constexpr int SPIN_CAP = 1 << 18;
constexpr unsigned TAG_STEP = 0x00010001u;
constexpr int AUX_NT = 2, AUX_SC1 = 16;
constexpr float L2E = 1.4426950408889634f, SCALE = 0.088388347648318440f;

#define LAY "tjjwwnjnjntjnt"
constexpr unsigned lay_vec(int i) {
  constexpr int cb[7] = {8, 9, 10, 11, 14, 12, 13};
  return LAY[14] == '_' ? 1u << (10 + i) : i < 5 ? 1u << cb[i + 2] : ((1u << (10 + i)) | (1u << cb[(i - 5) % 7])) & ~1023u;
}
template <char C> __device__ __forceinline__ unsigned deposit(unsigned v) {
  unsigned r = 0; int k = 0;
#pragma unroll
  for (int i = 0; i < 14; ++i) if (LAY[i] == C) { r ^= ((v >> k) & 1u) * lay_vec(i); ++k; }
  return r;
}
__device__ __forceinline__ float bflo(unsigned v) { return __uint_as_float(v << 16); }
__device__ __forceinline__ float bfhi(unsigned v) { return __uint_as_float(v & 0xffff0000u); }
__device__ __forceinline__ unsigned cvtpk(float lo, float hi) {
  bf16x2 r; r[0] = (__bf16)lo; r[1] = (__bf16)hi; return __builtin_bit_cast(unsigned, r);
}
__device__ __forceinline__ float rbf(float f) { return bflo(cvtpk(f, f)); }
template <int DC> __device__ __forceinline__ float dpp(float x) {
  return __builtin_bit_cast(float, __builtin_amdgcn_mov_dpp(__builtin_bit_cast(int, x), DC, 0xf, 0xf, true));
}
constexpr int ROR1 = 0x121, ROR2 = 0x122, ROR4 = 0x124, ROR8 = 0x128, SHL4 = 0x104, SHL8 = 0x108, SHL12 = 0x10c;
__device__ __forceinline__ float xrow_max(float x) {
  auto s = __builtin_amdgcn_permlane16_swap(__float_as_uint(x), __float_as_uint(x), false, false);
  x = fmaxf(__uint_as_float(s[0]), __uint_as_float(s[1]));
  auto t = __builtin_amdgcn_permlane32_swap(__float_as_uint(x), __float_as_uint(x), false, false);
  return fmaxf(__uint_as_float(t[0]), __uint_as_float(t[1]));
}
__device__ __forceinline__ float xrow_sum(float x) {
  auto s = __builtin_amdgcn_permlane16_swap(__float_as_uint(x), __float_as_uint(x), false, false);
  x = __uint_as_float(s[0]) + __uint_as_float(s[1]);
  auto t = __builtin_amdgcn_permlane32_swap(__float_as_uint(x), __float_as_uint(x), false, false);
  return __uint_as_float(t[0]) + __uint_as_float(t[1]);
}
__device__ __forceinline__ float row_sum(float x) {
  x += dpp<ROR8>(x); x += dpp<ROR4>(x); x += dpp<ROR2>(x); x += dpp<ROR1>(x); return x;
}
__device__ __forceinline__ float wave_sum(float x) { return xrow_sum(row_sum(x)); }
__device__ __forceinline__ float dot2(unsigned a, unsigned b, float c) {
  return __builtin_amdgcn_fdot2_f32_bf16(__builtin_bit_cast(bf16x2, a), __builtin_bit_cast(bf16x2, b), c, false);
}
__device__ __forceinline__ unsigned perm_lo(unsigned hi, unsigned lo) { return __builtin_amdgcn_perm(hi, lo, 0x05040100u); }
__device__ __forceinline__ unsigned perm_hi(unsigned hi, unsigned lo) { return __builtin_amdgcn_perm(hi, lo, 0x07060302u); }
__device__ __forceinline__ unsigned colpair(const u32x4& r0, const u32x4& r1, int c) {
  return (c & 1) ? perm_hi(r1[c >> 1], r0[c >> 1]) : perm_lo(r1[c >> 1], r0[c >> 1]);
}
__device__ __forceinline__ rsrc_t mkrsrc(const void* p, unsigned bytes) { return __builtin_amdgcn_make_buffer_rsrc(const_cast<void*>(p), 0, bytes, 0x00020000); }
__device__ __forceinline__ f32x4 mfma(u32x4 a, u32x4 b, f32x4 c) {
  return __builtin_amdgcn_mfma_f32_16x16x32_bf16(__builtin_bit_cast(bf16x8, a), __builtin_bit_cast(bf16x8, b), c, 0, 0, 0);
}
__device__ __forceinline__ void fold8(const float (&a)[8], float (&c2)[2]) {
  float b4[4];
#pragma unroll
  for (int k = 0; k < 4; ++k) { auto r = __builtin_amdgcn_permlane32_swap(__float_as_uint(a[k]), __float_as_uint(a[k + 4]), false, false); b4[k] = __uint_as_float(r[0]) + __uint_as_float(r[1]); }
#pragma unroll
  for (int k = 0; k < 2; ++k) { auto r = __builtin_amdgcn_permlane16_swap(__float_as_uint(b4[k]), __float_as_uint(b4[k + 2]), false, false); c2[k] = __uint_as_float(r[0]) + __uint_as_float(r[1]); }
}

__device__ __forceinline__ u32x2 pack2(float a, float b, unsigned tag) {
  return u32x2{__builtin_amdgcn_perm(__float_as_uint(a), __float_as_uint(b), 0x07060503u), __builtin_amdgcn_perm(__float_as_uint(b), tag, 0x06050100u)};
}
__device__ __forceinline__ float unpackA(unsigned w0) { return __uint_as_float(w0); }
__device__ __forceinline__ float unpackB(unsigned w0, unsigned w1) { return __uint_as_float(__builtin_amdgcn_perm(w0, w1, 0x0403020cu)); }
__device__ __forceinline__ f32x2 sum_pieces(const u32x4 (&p)[4]) {
  float v[4];
  v[0] = (unpackA(p[0][0]) + unpackA(p[1][0])) + (unpackA(p[2][0]) + unpackA(p[3][0]));
  v[1] = (unpackB(p[0][0], p[0][1]) + unpackB(p[1][0], p[1][1])) + (unpackB(p[2][0], p[2][1]) + unpackB(p[3][0], p[3][1]));
  v[2] = (unpackA(p[0][2]) + unpackA(p[1][2])) + (unpackA(p[2][2]) + unpackA(p[3][2]));
  v[3] = (unpackB(p[0][2], p[0][3]) + unpackB(p[1][2], p[1][3])) + (unpackB(p[2][2], p[2][3]) + unpackB(p[3][2], p[3][3]));
  f32x2 r;
#pragma unroll
  for (int k = 0; k < 2; ++k) {
    auto t = __builtin_amdgcn_permlane32_swap(__float_as_uint(v[k]), __float_as_uint(v[k + 2]), false, false);
    float x = __uint_as_float(t[0]) + __uint_as_float(t[1]);
    x += dpp<ROR8>(x);
    auto u = __builtin_amdgcn_permlane16_swap(__float_as_uint(x), __float_as_uint(x), false, false);
    r[k] = __uint_as_float(u[0]) + __uint_as_float(u[1]);
  }
  return r;
}

template <int SLP>
__device__ __forceinline__ void gather4(u32x4 (&a)[4], const char* base, unsigned off, unsigned tag) {
  u64 m0, m1, m2, m3, t0; int cnt = SPIN_CAP;
  asm volatile(
      "s_mov_b64 %[m0], -1\n\ts_mov_b64 %[m1], -1\n\ts_mov_b64 %[m2], -1\n\ts_mov_b64 %[m3], -1\n\ts_nop 0\n"
      "0:\n\t"
      "s_wqm_b64 exec, %[m0]\n\tglobal_load_dwordx4 v[40:43], %[off], %[base] sc1\n\t"
      "s_wqm_b64 exec, %[m1]\n\tglobal_load_dwordx4 v[44:47], %[off], %[base] offset:1024 sc1\n\t"
      "s_wqm_b64 exec, %[m2]\n\tglobal_load_dwordx4 v[48:51], %[off], %[base] offset:2048 sc1\n\t"
      "s_wqm_b64 exec, %[m3]\n\tglobal_load_dwordx4 v[52:55], %[off], %[base] offset:3072 sc1\n\t"
      "s_mov_b64 exec, -1\n\t"
      "s_sub_u32 %[cnt], %[cnt], 1\n\ts_cbranch_scc1 9f\n\t"
      "s_waitcnt vmcnt(0)\n\t"
      "v_perm_b32 v56, v43, v41, %[sel]\n\tv_perm_b32 v57, v47, v45, %[sel]\n\tv_perm_b32 v58, v51, v49, %[sel]\n\tv_perm_b32 v59, v55, v53, %[sel]\n\t"
      "v_cmp_ne_u32 %[m0], %[tag], v56\n\tv_cmp_ne_u32 %[m1], %[tag], v57\n\tv_cmp_ne_u32 %[m2], %[tag], v58\n\tv_cmp_ne_u32 %[m3], %[tag], v59\n\t"
      ".if %c[slp]\n\ts_sleep %c[slp]\n\t.endif\n\t"
      "s_or_b64 %[t0], %[m0], %[m1]\n\ts_or_b64 vcc, %[m2], %[m3]\n\ts_or_b64 %[t0], %[t0], vcc\n\t"
      "s_cbranch_scc1 0b\n"
      "9:\n\ts_waitcnt vmcnt(0)"
      : "=&{v[40:43]}"(a[0]), "=&{v[44:47]}"(a[1]), "=&{v[48:51]}"(a[2]), "=&{v[52:55]}"(a[3]),
        [m0] "=&s"(m0), [m1] "=&s"(m1), [m2] "=&s"(m2), [m3] "=&s"(m3), [t0] "=&s"(t0), [cnt] "+s"(cnt)
      : [tag] "s"(tag), [base] "s"(base), [off] "v"(off), [sel] "s"(0x05040100u), [slp] "i"(SLP)
      : "memory", "vcc", "scc", "v56", "v57", "v58", "v59");
}
__device__ __forceinline__ unsigned rot16(unsigned t) { return (t >> 16) | (t << 16); }
__device__ __forceinline__ u32x4 piece(unsigned a, unsigned b, unsigned tag) { return u32x4{a, tag, b, rot16(tag)}; }
__device__ __forceinline__ u32x4 load_dev(const char* base, unsigned off) {
  u32x4 a;
  asm volatile("global_load_dwordx4 %0, %1, %2 sc1\n\ts_waitcnt vmcnt(0)" : "=&v"(a) : "v"(off), "s"(base) : "memory");
  return a;
}
__device__ __forceinline__ u32x4 gather1(const char* base, unsigned off, unsigned tag) {
  u32x4 p;
#pragma unroll 1
  for (int spin = 0; spin < SPIN_CAP; ++spin) {
    p = load_dev(base, off);
    if (__builtin_amdgcn_ballot_w64(((p[1] ^ tag) | (p[3] ^ rot16(tag))) != 0) == 0) break;
    __builtin_amdgcn_s_sleep(32);
  }
  return p;
}
template <int SLP>
__device__ __forceinline__ void lds_wait(const unsigned* flags, int idx) {
#pragma unroll 1
  for (int spin = 0; spin < 32 * SPIN_CAP; ++spin) {
    if (__builtin_amdgcn_ballot_w64(*(volatile LAS unsigned*)(flags + idx) == 0u) == 0) break;
    __builtin_amdgcn_s_sleep(SLP);
  }
  asm volatile("" ::: "memory");
}
__device__ __forceinline__ void lds_set(unsigned* flag) {
  asm volatile("" ::: "memory");
  *(volatile LAS unsigned*)flag = 1u;
}
__device__ __forceinline__ int timed_read(const char* p) {
  u64 t0, t1; unsigned d;
  asm volatile("s_memtime %0\n\ts_waitcnt lgkmcnt(0)\n\ts_load_dword %2, %3, 0\n\ts_waitcnt lgkmcnt(0)\n\ts_memtime %1\n\ts_waitcnt lgkmcnt(0)"
               : "=&s"(t0), "=&s"(t1), "=&s"(d) : "s"(p) : "memory");
  return (int)((unsigned)t1 - (unsigned)t0);
}

#define SB() __builtin_amdgcn_sched_barrier(0)
#define SAME_PAGE(n) do { SB(); asm volatile("s_branch 1f\n\t.p2align 12,," #n "\n1:"); SB(); } while (0)
#define PIN(x) asm volatile("" : "+v"(x))
#define PACE(w) do { SB(); asm volatile("s_waitcnt vmcnt(%0)" :: "i"((w) - 1) : "memory"); } while (0)
#define PACEV(t) do { SB(); asm volatile("s_swappc_b64 s[96:97], %0" :: "s"(tgt[(t) & 1]) : "memory", "s96", "s97"); } while (0)
#define VMDONE() __builtin_amdgcn_s_waitcnt(0x0f70)
#define BARRIER() do { SB(); asm volatile("s_waitcnt lgkmcnt(0)\n\ts_barrier" ::: "memory"); SB(); } while (0)

__global__ void __launch_bounds__(512, 2) __attribute__((aligned(4096)))
fused_decode(const void* K_, const void* V_, void* ws_, const void* x_, const void* wq_, const void* w1_, const void* wo_, const void* w2_, void* out_) {
  __shared__ __attribute__((aligned(16))) float lds[2048 + 16 * ACCH + 128 + 64 + 4 * NT * 256];
  float* const sm_q = lds;
  float* const sm_acc = lds + 2048;
  float* const sm_o = sm_acc + 16 * ACCH;
  float* const sm_m = sm_o + 128;
  float* const sm_lse = sm_m + 16;
  unsigned* const sm_w = reinterpret_cast<unsigned*>(sm_lse + 8);
  unsigned* const sm_flag = sm_w + 8;
  float* const sm_s = sm_m + 64;
  asm volatile("s_load_dwordx16 s[64:79], %0, 0\n\ts_load_dwordx2 s[80:81], %0, 0x40" :: "s"(__builtin_amdgcn_kernarg_segment_ptr())
               : "s64", "s65", "s66", "s67", "s68", "s69", "s70", "s71", "s72", "s73", "s74", "s75", "s76", "s77", "s78", "s79", "s80", "s81");
  SB();
  const int b = blockIdx.x, g = b & 7, j = b >> 3;
  const int tid = threadIdx.x, lane = tid & 63, wave = __builtin_amdgcn_readfirstlane(tid >> 6);
  const int lg = lane >> 4, li = lane & 15;
  unsigned koff = (li >> 2) * 256 + lg * 64 + (li & 3) * 16, voff = lane * 16;
  const unsigned wbase = deposit<'j'>(j) ^ deposit<'w'>(wave);
  unsigned qoff = (lane >> 3) * 16 * 8192 + (lane & 7) * 16, xoff = (lane >> 3) * 32, w1off = (lane >> 3) * 8 * (DFF * 2) + (lane & 7) * 16;
  if (tid < 24) sm_flag[tid] = 0u;
  u64x8 ka; u64 ka8;
  {
    u64 pc;
    asm volatile("s_cmp_lg_u32 %9, 7\n\ts_cbranch_scc1 1f\n\ts_getpc_b64 %2\n\ts_lshr_b64 %2, %2, 12\n\ts_lshl_b64 %2, %2, 12\n\tglobal_load_dword v255, %8, %2\n1:\n\t"
                 "s_waitcnt lgkmcnt(0)"
                 : "=&{s[64:79]}"(ka), "=&{s[80:81]}"(ka8), "=&s"(pc), "+v"(koff), "+v"(voff), "+v"(qoff), "+v"(xoff), "+v"(w1off)
                 : "v"(min(j * 1024 + (lane & 7) * 128, CODE_BYTES - 4)), "s"(wave) : "memory", "scc", "v255");
  }
  const char *K = (const char*)ka[0], *V = (const char*)ka[1], *x = (const char*)ka[3], *wq = (const char*)ka[4], *w1 = (const char*)ka[5], *wo = (const char*)ka[6], *w2 = (const char*)ka[7];
  char *ws = (char*)((ka[2] + 0x1fffffull) & ~0x1fffffull), *out = (char*)ka8;
  const int hw = wave - 4;
  const rsrc_t rx = mkrsrc(x, DM * 2);
  const rsrc_t rw = mkrsrc(ws, WS_END);
  unsigned tag = 0;

  u32x4 xr, xa, xb, xc, wr[16], ur[8];
  unsigned epoch;
  const int item = g * 28 + j;
  if (wave == 4) {
    epoch = __builtin_amdgcn_raw_buffer_load_b32(rw, 0, WS_EPOCH, AUX_SC1);
    xr = __builtin_amdgcn_raw_buffer_load_b128(rx, voff, 0, 0);
    xa = __builtin_amdgcn_raw_buffer_load_b128(rx, xoff, (j >> 3) * 256, 0); xb = __builtin_amdgcn_raw_buffer_load_b128(rx, xoff + 16, (j >> 3) * 256, 0);
    const rsrc_t rq = mkrsrc(wq, DM * 8192), r1 = mkrsrc(w1, j < 28 ? DM * DFF * 2 : 0);
#pragma unroll
    for (int i = 0; i < 16; ++i) wr[i] = __builtin_amdgcn_raw_buffer_load_b128(rq, qoff, (128 * (j >> 3) + i) * 8192 + g * 1024 + (j & 7) * 128, AUX_NT);
    SB();
#pragma unroll
    for (int i = 0; i < 8; ++i) ur[i] = __builtin_amdgcn_raw_buffer_load_b128(r1, w1off, (64 * (item & 7) + i) * (DFF * 2) + (item >> 3) * 128, AUX_NT);
  }
  SB();
  asm volatile("s_barrier" ::: "memory");
  SB();

  const rsrc_t rk = mkrsrc(K + (size_t)g * HEADB, HEADB), rv = mkrsrc(V + (size_t)g * HEADB, HEADB);
  auto toff = [&](int n) -> unsigned { return wbase ^ deposit<'n'>(n); };
  auto ldk = [&](unsigned so, int t) -> u32x4 { return __builtin_amdgcn_raw_buffer_load_b128(rk, koff, so ^ deposit<'t'>(t), AUX_NT); };
  auto ldv = [&](unsigned so, int t) -> u32x4 { return __builtin_amdgcn_raw_buffer_load_b128(rv, voff, so ^ deposit<'t'>(t), AUX_NT); };
  u32x4 ring[4][8];
  int role = wave >> 2;
  asm volatile("" : "+s"(role));
  if (role == 0) {
#define KFIRST(i0, i1) _Pragma("unroll") for (int i = i0; i < i1; ++i) { \
      if (i >= WIN) PACE(WIN); \
      ring[i >> 3][i & 7] = ldk(toff(i >> 3), i & 7); SB(); }
    KFIRST(0, QPOS)
    lds_wait<1>(sm_flag, lane & 3);
    KFIRST(QPOS, QPOS + 1)
    f32x4 qa[4], qc[4];
    {
      const float* qp = sm_q + (li & 3) * 128 + 8 * (4 * lg + (li >> 2));
#pragma unroll
      for (int r = 0; r < 4; ++r) { qa[r] = *reinterpret_cast<const f32x4*>(qp + 512 * r); qc[r] = *reinterpret_cast<const f32x4*>(qp + 512 * r + 4); }
    }
    const unsigned tagv = sm_w[0];
    KFIRST(QPOS + 1, QPOS + 2)
    u32x4 qb;
    {
      const f32x4 a = ((qa[0] + qa[1]) + qa[2]) + qa[3], c = ((qc[0] + qc[1]) + qc[2]) + qc[3];
      qb = u32x4{cvtpk(a[0], a[1]), cvtpk(a[2], a[3]), cvtpk(c[0], c[1]), cvtpk(c[2], c[3])};
      PIN(qb);
    }
    tag = __builtin_amdgcn_readfirstlane(tagv);
    KFIRST(QPOS + 2, 32)
#undef KFIRST
    u64 tbl, tgt[2];
    asm volatile("s_getpc_b64 %0\n\ts_branch 1f\n\t.set w, 0\n\t.rept 16\n\ts_waitcnt vmcnt(w)\n\ts_setpc_b64 s[96:97]\n\t.set w, w+1\n\t.endr\n1:" : "=s"(tbl));
    auto steer = [&](int n) {
      const unsigned now = (unsigned)__builtin_amdgcn_s_memrealtime();
      const unsigned lag = now - __builtin_amdgcn_readfirstlane(*(volatile LAS unsigned*)(sm_w + 1)) - (unsigned)((n * CP) >> 1);
      const int w2 = min(max(CW0 + ((int)(lag * CG) >> 8), CMIN), CMAX);
      tgt[0] = tbl + (unsigned)(8 * (w2 >> 1) - 4); tgt[1] = tbl + (unsigned)(8 * ((w2 + 1) >> 1) - 4);
    };
    *(volatile LAS unsigned*)(sm_flag + 12 + wave) = (unsigned)__builtin_amdgcn_s_memrealtime() | 1u;
    u32x4* const sp = reinterpret_cast<u32x4*>(sm_s) + wave * (NT * 64) + lane;
    float mraw = -3.0e38f;
#pragma unroll 1
    for (int it = 0; it < NT; it += 4) {
      const bool last = it == NT - 4;
      const rsrc_t rn = mkrsrc(last ? V + (size_t)g * HEADB : K + (size_t)g * HEADB, HEADB);
      const unsigned noff = last ? voff : koff;
#pragma unroll
      for (int u = 0; u < 4; ++u) {
        steer(it + u);
        const unsigned so = toff((it + u + 4) & (NT - 1));
        u32x4 sc;
        float v[2];
#pragma unroll
        for (int t = 0; t < 8; ++t) {
          const f32x4 d = mfma(ring[u][t], qb, f32x4{0, 0, 0, 0});
          PACEV(t);
          ring[u][t] = __builtin_amdgcn_raw_buffer_load_b128(rn, noff, so ^ deposit<'t'>(t), AUX_NT);
          SB();
          v[t & 1] = ((d[0] + dpp<SHL4>(d[1])) + dpp<SHL8>(d[2])) + dpp<SHL12>(d[3]);
          if (t & 1) {
            const unsigned r = cvtpk(v[0], v[1]);
            sc[t >> 1] = cvtpk(bflo(r) * SCALE, bfhi(r) * SCALE);
            mraw = fmaxf(mraw, fmaxf(v[0], v[1]));
          }
        }
        sp[(it + u) * 64] = sc;
      }
    }
    u32x4 f0[8];
#pragma unroll
    for (int e = 0; e < 8; ++e) {
#pragma unroll
      for (int k = 0; k < 4; ++k) f0[e][k] = colpair(ring[0][2 * k], ring[0][2 * k + 1], e);
      PIN(f0[e]);
    }
#define V4STEP(t) do { PACEV(t); ring[0][t] = ldv(toff(4), t); SB(); } while (0)
    V4STEP(0);
    steer(16);
    mraw = rbf(rbf(xrow_max(mraw)) * SCALE);
    if (lane < 4) sm_m[wave * 4 + lane] = mraw;
    lds_set(sm_flag + 4 + wave);
    float mref = *(volatile LAS float*)(sm_m + wave * 4 + (li & 3)), lg2 = 0.f;
    const u32x4* const sr = sp - li + (li & 3);
    f32x4 acc[2] = {{0, 0, 0, 0}, {0, 0, 0, 0}};
    auto probs = [&](unsigned sc) -> unsigned {
      return cvtpk(__builtin_amdgcn_exp2f(__builtin_fmaf(bflo(sc) - mref, L2E, -lg2)), __builtin_amdgcn_exp2f(__builtin_fmaf(bfhi(sc) - mref, L2E, -lg2)));
    };
    auto only = [&](const u32x4& p, int sx) -> u32x4 { return (li >> 2) == sx ? p : u32x4{0u, 0u, 0u, 0u}; };
    auto pv = [&](const u32x4 (&vc)[8], int n, u32x4 (&vn)[8], int nn, auto t0, auto ns) {
      constexpr int T0 = decltype(t0)::value, NS = decltype(ns)::value;
      const unsigned so = toff(nn);
#define VSTEP(i) do { if constexpr (NS && ((i) * NS) % 8 == 0) { constexpr int t = T0 + (i) * NS / 8; PACEV(t); vn[t] = ldv(so, t); SB(); } } while (0)
      const u32x4 sc = sr[n * 64];
      VSTEP(0);
      u32x4 pb;
      pb[0] = probs(sc[0]); pb[1] = probs(sc[1]);
      VSTEP(1);
      pb[2] = probs(sc[2]); pb[3] = probs(sc[3]);
      VSTEP(2);
      VSTEP(3);
      auto two = [&](int sx) {
        const u32x4 ps = only(pb, sx);
#pragma unroll
        for (int a = 0; a < 2; ++a) {
          u32x4 f;
#pragma unroll
          for (int k = 0; k < 4; ++k) f[k] = colpair(vc[2 * k], vc[2 * k + 1], 4 * a + sx);
          acc[a] = mfma(f, ps, acc[a]);
        }
      };
      two(0); VSTEP(4); two(1); VSTEP(5); two(2); VSTEP(6); two(3); VSTEP(7);
#undef VSTEP
    };
    using I0 = std::integral_constant<int, 0>;
    using I4 = std::integral_constant<int, 4>;
    using I8 = std::integral_constant<int, 8>;
    {
      const u32x4 sc = sr[0];
      V4STEP(1);
      u32x4 pb;
#pragma unroll
      for (int k = 0; k < 4; ++k) pb[k] = probs(sc[k]);
      V4STEP(2);
#pragma unroll
      for (int sx = 0; sx < 4; ++sx) { const u32x4 ps = only(pb, sx); acc[0] = mfma(f0[sx], ps, acc[0]); acc[1] = mfma(f0[4 + sx], ps, acc[1]); }
      V4STEP(3);
    }
#undef V4STEP
    pv(ring[1], 1, ring[0], 4, I4{}, I4{});
#pragma unroll 1
    for (int it = 2; it < NT - 6; it += 4) {
      steer(15 + it);
      pv(ring[2], it, ring[1], it + 3, I0{}, I8{});
      steer(16 + it);
      pv(ring[3], it + 1, ring[2], it + 4, I0{}, I8{});
      steer(17 + it);
      pv(ring[0], it + 2, ring[3], it + 5, I0{}, I8{});
      steer(18 + it);
      pv(ring[1], it + 3, ring[0], it + 6, I0{}, I8{});
    }
    steer(25);
    pv(ring[2], 10, ring[1], 13, I0{}, I8{});
    steer(26);
    lds_wait<1>(sm_flag, 10 + (lane & 1));
    {
      const f32x2 gl = *reinterpret_cast<const f32x2*>(sm_lse + 2 * (li & 3));
      const float fs = __builtin_amdgcn_exp2f(__builtin_fmaf(mref - gl[0], L2E, -gl[1]));
      mref = gl[0]; lg2 = gl[1];
      acc[0] *= fs; acc[1] *= fs;
    }
    pv(ring[3], 11, ring[2], 14, I0{}, I8{});
    const unsigned half = (sm_w[2] >> g) & 1u;
    unsigned a_wr = (unsigned)(uintptr_t)(LAS float*)(sm_acc + (wave * 4 + (li & 3)) * ACCH + lg * ACCL + 4 * (li >> 2));
    unsigned a_rd = (unsigned)(uintptr_t)(LAS float*)(sm_acc + wave * ACCH + 2 * lane);
    unsigned a_o = (unsigned)(uintptr_t)(LAS float*)(sm_o + 2 * (lane & 7) + (lane >> 5));
    unsigned a_g = blk(WS_E1, g * 16 + wave * 4 + lg, half) + j * 128 + li * 8, a_in = blk(WS_E1, g * 16 + (j & 15), half) + lane * 16;
    unsigned tagl = tag >> (16 * (li & 1));
    PIN(a_wr); PIN(a_rd); PIN(a_o); PIN(a_g); PIN(a_in); PIN(tagl);
    steer(27);
    pv(ring[0], 12, ring[3], 15, I0{}, I4{});
    SAME_PAGE(2900);
    pv(ring[1], 13, ring[3], 15, I4{}, I4{});
    lds_set(sm_flag + 16 + wave);
    pv(ring[2], 14, ring[3], 0, I0{}, I0{});
    {
      const u32x4 sc = sr[(NT - 1) * 64];
      u32x4 pb, ps[4], f[8];
#pragma unroll
      for (int k = 0; k < 4; ++k) pb[k] = probs(sc[k]);
#pragma unroll
      for (int sx = 0; sx < 4; ++sx) ps[sx] = only(pb, sx);
#pragma unroll
      for (int k = 0; k < 3; ++k) {
        SB();
#pragma unroll
        for (int e = 0; e < 8; ++e) f[e][k] = colpair(ring[3][2 * k], ring[3][2 * k + 1], e);
      }
      SB();
#pragma unroll
      for (int e = 0; e < 8; ++e) { f[e][3] = colpair(ring[3][6], ring[3][7], e); acc[e >> 2] = mfma(f[e], ps[e & 3], acc[e >> 2]); }
    }
    *reinterpret_cast<LAS f32x4*>(a_wr) = acc[0];
    *reinterpret_cast<LAS f32x4*>(a_wr + 64) = acc[1];
    BARRIER();
    {
      const f32x2 v = (*reinterpret_cast<const LAS f32x2*>(a_rd) + *reinterpret_cast<const LAS f32x2*>(a_rd + 16 * ACCH)) +
                      (*reinterpret_cast<const LAS f32x2*>(a_rd + 32 * ACCH) + *reinterpret_cast<const LAS f32x2*>(a_rd + 48 * ACCH));
      __builtin_amdgcn_raw_buffer_store_b64(pack2(v[0], v[1], tagl), rw, a_g, 0, AUX_SC1);
    }
    if (wave == 2 && j < 16) {
      __builtin_amdgcn_s_sleep(E1DELAY);
      u32x4 pa[4];
      gather4<0>(pa, ws, a_in, tag);
      const f32x2 o = sum_pieces(pa);
      *reinterpret_cast<LAS unsigned*>(a_o) = cvtpk(o[0], o[1]);
    }
    BARRIER();
    asm volatile("s_endpgm" : "=v"(wr[0]), "=v"(wr[1]), "=v"(wr[2]), "=v"(wr[3]), "=v"(wr[4]), "=v"(wr[5]), "=v"(wr[6]), "=v"(wr[7]), "=v"(wr[8]), "=v"(wr[9]),
                 "=v"(wr[10]), "=v"(wr[11]), "=v"(wr[12]), "=v"(wr[13]), "=v"(wr[14]), "=v"(wr[15]), "=v"(xr), "=v"(xa), "=v"(xb), "=v"(epoch),
                 "=v"(ur[0]), "=v"(ur[1]), "=v"(ur[2]), "=v"(ur[3]), "=v"(ur[4]), "=v"(ur[5]), "=v"(ur[6]), "=v"(ur[7]) :: "memory");
  }

  if (wave == 4) {
    tag = __builtin_amdgcn_readfirstlane(epoch) + TAG_STEP;
    SB();
    const unsigned t0 = (unsigned)__builtin_amdgcn_s_memrealtime();
    *reinterpret_cast<u32x2*>(sm_w) = u32x2{tag, t0 + CT0};
    xc = __builtin_amdgcn_raw_buffer_load_b128(rx, xoff >> 1, (item & 7) * 128, 0);
    float xf[8];
#pragma unroll
    for (int k = 0; k < 4; ++k) { xf[2 * k] = bflo(xr[k]); xf[2 * k + 1] = bfhi(xr[k]); }
    const float mean = wave_sum(((xf[0] + xf[1]) + (xf[2] + xf[3])) + ((xf[4] + xf[5]) + (xf[6] + xf[7]))) * (1.f / 512.f);
    float vs = 0.f;
#pragma unroll
    for (int k = 0; k < 8; ++k) { const float d = xf[k] - mean; vs += d * d; }
    const float var = wave_sum(vs) * (1.f / 512.f);
    const float mb = rbf(mean), rden = __builtin_amdgcn_rcpf(rbf(__builtin_amdgcn_sqrtf(rbf(rbf(var) + 1e-5f))));
    auto ln2 = [&](unsigned xp) -> unsigned { return cvtpk(rbf(bflo(xp) - mb) * rden, rbf(bfhi(xp) - mb) * rden); };
    auto send = [&](const float (&a)[8], unsigned dst) {
      float c2[2];
      fold8(a, c2);
      c2[0] += dpp<ROR8>(c2[0]); c2[1] += dpp<ROR8>(c2[1]);
      if (li < 8) __builtin_amdgcn_raw_buffer_store_b128(piece(__float_as_uint(c2[0]), __float_as_uint(c2[1]), tag), rw, lg * 128 + li * 16, dst, AUX_SC1);
    };
    float a[8] = {0, 0, 0, 0, 0, 0, 0, 0}, u[8] = {0, 0, 0, 0, 0, 0, 0, 0};
#pragma unroll
    for (int k = 0; k < 8; ++k) {
      const unsigned xn = ln2(k < 4 ? xa[k & 3] : xb[k & 3]);
#pragma unroll
      for (int c = 0; c < 8; ++c) a[c] = dot2(colpair(wr[2 * k], wr[2 * k + 1], c), xn, a[c]);
    }
    send(a, WS_Q + g * 16384 + (j >> 3) * 4096 + (j & 7) * 512);
    lds_set(sm_flag + 8);
    SB();
#pragma unroll
    for (int k = 0; k < 4; ++k) {
      const unsigned xn = ln2(xc[k]);
#pragma unroll
      for (int c = 0; c < 8; ++c) u[c] = dot2(colpair(ur[2 * k], ur[2 * k + 1], c), xn, u[c]);
    }
    send(u, j < 28 ? WS_H + (item & 7) * 14336 + (item >> 3) * 512 : WS_END);
  }
  int vote = 0;
  auto sample = [&](int k) {
    const char* p = ws + WS_P + k * 16384 + (((j - 24 + 4 * k) & 7) * 8 + g) * 128;
    vote += min(max(timed_read(p) - timed_read(p + 8192), -600), 600);
  };
  if (hw == 3 && j >= 24) {
    timed_read(ws + WS_P + 7 * 16384);
    sample(0);
  }
  if (hw == 2) {
    const char* p = (j & 8 ? V : K) + (size_t)g * HEADB + (j & 7) * 2097152u;
    if (j >= 16) p = j == 16 ? wo + g * 524288 : j == 17 ? w2 + g * 229376 : j == 18 ? out : (j & 1 ? V : K) + (size_t)g * HEADB + (HEADB - 4);
    timed_read(p);
  }
  lds_wait<1>(sm_flag, 8);
  tag = __builtin_amdgcn_readfirstlane(sm_w[0]);
  if (hw) __builtin_amdgcn_s_sleep(QSLEEP); else __builtin_amdgcn_s_sleep(QSLEEP0);
  {
    u32x4 pq[4];
    gather4<0>(pq, ws, WS_Q + g * 16384 + hw * 4096 + lane * 16, tag);
#pragma unroll
    for (int i = 0; i < 4; ++i) *reinterpret_cast<u32x2*>(sm_q + hw * 512 + i * 128 + (lane >> 5) * 64 + (lane & 7) * 8 + ((lane >> 3) & 3) * 2) = u32x2{pq[i][0], pq[i][2]};
    lds_set(sm_flag + hw);
  }
  const int f = (j - 16) * 8 + g;
  if (hw == 3) {
    if (j >= 24) {
#pragma unroll 1
      for (int k = 1; k < 3; ++k) sample(k);
    }
    lds_wait<4>(sm_flag, 12);
    const unsigned t1 = sm_flag[12];
    if (lane < 4 && j >= 24) __builtin_amdgcn_raw_buffer_store_b128(piece((unsigned)vote, t1, tag), rw, lane * 16, WS_V + (g * 8 + j - 24) * 128, AUX_SC1);
    __builtin_amdgcn_s_sleep(VSLEEP);
    const u32x4 vt = gather1(ws, WS_V + lane * 128, tag);
    *(volatile LAS unsigned*)(sm_w + 1) = t1 + COFF + (int)(wave_sum((float)(int)(vt[2] - t1)) * (1.f / 64.f));
    int d = (int)vt[0];
    d += __builtin_amdgcn_mov_dpp(d, 0xB1, 0xf, 0xf, true); d += __builtin_amdgcn_mov_dpp(d, 0x4E, 0xf, 0xf, true); d += __builtin_amdgcn_mov_dpp(d, 0x141, 0xf, 0xf, true);
    const unsigned halves = (unsigned)(((__builtin_amdgcn_ballot_w64(d > 0) & 0x0101010101010101ull) * 0x0102040810204080ull) >> 56);
    sm_w[2] = halves;
    lds_set(sm_flag + 11);
  }
  if (j < 18 && (hw == 3 || j >= 16)) {
    lds_wait<8>(sm_flag, 11);
    const char* p = ws + blk(j < 16 ? WS_E1 : WS_E2, j < 16 ? g * 16 + j : f * 4 + hw, (__builtin_amdgcn_readfirstlane(sm_w[2]) >> g) & 1u);
#pragma unroll 1
    for (int i = 0; i < 4096; i += 512) {
      unsigned t;
      asm volatile(".set o, 0\n\t.rept 8\n\ts_load_dword %0, %1, o\n\t.set o, o + 64\n\t.endr\n\ts_waitcnt lgkmcnt(0)" : "=&s"(t) : "s"(p + i) : "memory");
    }
  }
  unsigned hk[2] = {0u, 0u};
  const int ub = g * 14 + j;
  if (j < 14) {
    if (hw != 3) __builtin_amdgcn_s_sleep(HSLEEP);
    const int u = ub * 16 + 4 * lg + 2 * (li & 1);
    const u32x4 ph = gather1(ws, WS_H + (li >> 1) * 14336 + (u >> 6) * 512 + ((u >> 1) & 3) * 128 + ((u >> 3) & 7) * 16, tag);
    float h0 = __uint_as_float(ph[0]), h1 = __uint_as_float(ph[2]);
    h0 += dpp<ROR8>(h0); h1 += dpp<ROR8>(h1); h0 += dpp<ROR4>(h0); h1 += dpp<ROR4>(h1); h0 += dpp<ROR2>(h0); h1 += dpp<ROR2>(h1);
    const float hp = __uint_as_float(cvtpk(fmaxf(h0, 0.f), fmaxf(h1, 0.f)));
    hk[0] = __float_as_uint(dpp<0x00>(hp)); hk[1] = __float_as_uint(dpp<0x55>(hp));
  }
  if (hw == 2) {
    lds_wait<8>(sm_flag, 4 + (lane & 3));
    const int h = lane & 3;
    const float M = fmaxf(fmaxf(sm_m[h], sm_m[4 + h]), fmaxf(sm_m[8 + h], sm_m[12 + h]));
    const u32x4* sp = reinterpret_cast<const u32x4*>(sm_s) + lg * 64 + (li >> 2) * 16 + h;
    float l = 0.f;
#pragma unroll 2
    for (int i = 0; i < NT; ++i) {
      const u32x4 sc = sp[i * 256];
#pragma unroll
      for (int k = 0; k < 4; ++k) l += __builtin_amdgcn_exp2f((bflo(sc[k]) - M) * L2E) + __builtin_amdgcn_exp2f((bfhi(sc[k]) - M) * L2E);
    }
    l += dpp<ROR8>(l); l += dpp<ROR4>(l);
    l = xrow_sum(l);
    if (lane < 8) __builtin_amdgcn_raw_buffer_store_b128(piece(__float_as_uint(M), __float_as_uint(l), tag), rw, lane * 16, WS_S + (g * 32 + j) * 128, AUX_SC1);
    __builtin_amdgcn_s_sleep(LSLEEP);
    u32x4 pl[4];
    gather4<8>(pl, ws, WS_S + g * 4096 + lane * 16, tag);
    float G = fmaxf(fmaxf(__uint_as_float(pl[0][0]), __uint_as_float(pl[1][0])), fmaxf(__uint_as_float(pl[2][0]), __uint_as_float(pl[3][0])));
    G = xrow_max(fmaxf(G, dpp<ROR8>(G)));
    float L = 0.f;
#pragma unroll
    for (int i = 0; i < 4; ++i) L += __uint_as_float(pl[i][2]) * __builtin_amdgcn_exp2f((__uint_as_float(pl[i][0]) - G) * L2E);
    L = xrow_sum(L + dpp<ROR8>(L));
    if (lane < 4) *reinterpret_cast<f32x2*>(sm_lse + 2 * lane) = f32x2{G, __builtin_amdgcn_logf(L)};
    lds_set(sm_flag + 10);
  }
  if (j >= 18) {
    asm volatile("s_waitcnt lgkmcnt(0)" ::: "memory");
    VMDONE(); return;
  }
  if (j < 16) {
    u32x4 w2r[4], wor[8];
    const rsrc_t r2 = mkrsrc(w2, j < 14 ? DFF * DM * 2 : 0), ro = mkrsrc(wo, 4096 * DM * 2);
    SAME_PAGE(1400);
    lds_wait<1>(sm_flag, 16 + (lane & 3));
#pragma unroll
    for (int i = 0; i < 4; ++i) w2r[i] = __builtin_amdgcn_raw_buffer_load_b128(r2, lg * 4096 + li * 16, (ub * 16 + i) * 1024 + hw * 256, AUX_NT);
#pragma unroll
    for (int i = 0; i < 8; ++i)
      wor[i] = __builtin_amdgcn_raw_buffer_load_b128(ro, lg * 2048 + li * 16, (g * 512 + j * 32 + 8 * (i & 3) + (i >> 2)) * 1024 + hw * 256, AUX_NT);
    const int fc = 4 * hw + lg;
    unsigned a_g = blk(WS_E2, fc * 4 + (g >> 1), (sm_w[2] >> (fc & 7)) & 1u) + ((g & 1) * 16 + j) * 128 + li * 8;
    unsigned a_l = (unsigned)(uintptr_t)(LAS float*)(sm_o + 4 * lg);
    unsigned tagl = tag >> (16 * (li & 1));
    PIN(a_g); PIN(a_l); PIN(tagl);
    BARRIER();
    float y[8] = {0, 0, 0, 0, 0, 0, 0, 0};
#pragma unroll
    for (int k = 0; k < 2; ++k)
#pragma unroll
      for (int c = 0; c < 8; ++c) y[c] = dot2(colpair(w2r[2 * k], w2r[2 * k + 1], c), hk[k], y[c]);
    unsigned wp[4][8];
#pragma unroll
    for (int k = 0; k < 4; ++k)
#pragma unroll
      for (int c = 0; c < 8; ++c) { wp[k][c] = colpair(wor[2 * k], wor[2 * k + 1], c); PIN(wp[k][c]); }
#pragma unroll
    for (int c = 0; c < 8; ++c) PIN(y[c]);
    BARRIER();
    const u32x4 op = *reinterpret_cast<const LAS u32x4*>(a_l);
#pragma unroll
    for (int k = 0; k < 4; ++k)
#pragma unroll
      for (int c = 0; c < 8; ++c) y[c] = dot2(wp[k][c], op[k], y[c]);
    float c2[2];
    fold8(y, c2);
    __builtin_amdgcn_raw_buffer_store_b64(pack2(c2[0], c2[1], tagl), rw, a_g, 0, AUX_SC1);
  } else {
    const int fcol = 128 * (f >> 2) + 8 * li + 2 * (f & 3);
    const unsigned xo = __builtin_amdgcn_raw_buffer_load_b32(rx, fcol * 2, 0, 0);
    float x0 = bflo(xo), x1 = bfhi(xo);
    lds_wait<8>(sm_flag, 11);
    SAME_PAGE(1000);
    unsigned a_g = blk(WS_E2, f * 4 + hw, (sm_w[2] >> g) & 1u) + lane * 16, a_o = lane < 16 ? fcol * 2 : DM * 2;
    unsigned a_w = (unsigned)(uintptr_t)(LAS float*)(sm_o + hw * 32 + 4 * (lane & 7) + 2 * (lane >> 5)), a_r = (unsigned)(uintptr_t)(LAS float*)(sm_o + 2 * li);
    PIN(a_g); PIN(a_o); PIN(a_w); PIN(a_r); PIN(x0); PIN(x1);
    const rsrc_t rout = mkrsrc(out, hw == 0 ? DM * 2 : 0), rep = mkrsrc(ws, (hw == 1 && f == 0) ? 4 : 0);
    BARRIER();
    __builtin_amdgcn_s_sleep(E2DELAY);
    u32x4 pa[4];
    gather4<0>(pa, ws, a_g, tag);
    *reinterpret_cast<LAS f32x2*>(a_w) = sum_pieces(pa);
    BARRIER();
    f32x2 r[4];
#pragma unroll
    for (int w = 0; w < 4; ++w) r[w] = *reinterpret_cast<const LAS f32x2*>(a_r + w * 128);
    const f32x2 t = (r[0] + r[1]) + (r[2] + r[3]);
    __builtin_amdgcn_raw_buffer_store_b32(cvtpk(x0 + t[0], x1 + t[1]), rout, a_o, 0, 0);
    __builtin_amdgcn_raw_buffer_store_b32(tag, rep, lane * 4, WS_EPOCH, AUX_SC1);
  }
  VMDONE();
}

extern "C" void kernel_launch(void* const* d_in, const int* in_sizes, int n_in, void* d_out, int out_size, void* d_ws, size_t ws_size,
                              hipStream_t stream) {
  fused_decode<<<256, 512, 0, stream>>>(d_in[1], d_in[2], d_ws, d_in[0], d_in[3], d_in[6], d_in[4], d_in[7], d_out);
}
```
